```python
import torch

def _softmax(x, axis=-1):
    return torch.softmax(x, dim=axis)
def _gelu(x):
    return torch.nn.functional.gelu(x, approximate="tanh")
def _silu(x):
    return torch.nn.functional.silu(x)
def _sigmoid(x):
    return torch.sigmoid(x)
def _layernorm(x, axis=-1, eps=1e-5):
    m = x.mean(dim=axis, keepdim=True)
    v = x.var(dim=axis, keepdim=True, unbiased=False)
    return (x - m) / torch.sqrt(v + eps)
def _scatter_add(tgt, idx, src, axis=0):
    return tgt.clone().index_add_(axis, idx, src)
def _segment_sum(src, seg, n):
    out = torch.zeros((n,) + src.shape[1:], dtype=src.dtype)
    return out.index_add_(0, seg, src)
def _pad(x, pw):
    flat = tuple(v for pair in reversed(pw) for v in pair)
    return torch.nn.functional.pad(x, flat)

kernel_name = 'standalone__softmax_full_bidir__decode'

def setup_inputs():

    _w = (lambda a: (lambda t: t.to(torch.bfloat16) if t.is_floating_point() else t)(torch.as_tensor(a)))
    B, H, Sq, Skv, Dh = 2, 32, 1, 16384, 128
    return {
        "q": _w((lambda *s: torch.randn(*s, dtype=torch.bfloat16) * 0.8)(B, H, Sq, Dh)),
        "k": _w((lambda *s: torch.randn(*s, dtype=torch.bfloat16) * 0.8)(B, H, Skv, Dh)),
        "v": _w((lambda *s: torch.randn(*s, dtype=torch.bfloat16) * 0.05)(B, H, Skv, Dh)),
    }

def reference(q, k, v):
    scale = q.shape[-1] ** -0.5
    scores = torch.einsum('bhqd,bhkd->bhqk', q, k) * scale
    attn = _softmax(scores, axis=-1)
    return torch.einsum('bhqk,bhkd->bhqd', attn, v)
```

```cpp
#include <hip/hip_runtime.h>
#include <hip/hip_bf16.h>
#include <cstdint>

#define NSPLIT 8
#define WAVES 4
#define U 8

constexpr int D = 128;
constexpr int NH = 64;
constexpr int SKV = 16384;
constexpr int NTHR = WAVES * 64;
constexpr int TOK_PER_SPLIT = SKV / NSPLIT;
constexpr int TILE = 4 * U;
constexpr int NT = TOK_PER_SPLIT / (TILE * WAVES);
static_assert(NT * TILE * WAVES == TOK_PER_SPLIT, "");

using bf16x2 = __attribute__((ext_vector_type(2))) __bf16;
using u32x4 = __attribute__((ext_vector_type(4))) unsigned;
using f32x4 = __attribute__((ext_vector_type(4))) float;

__device__ __forceinline__ float dot2(unsigned k, unsigned q, float acc) {
  return __builtin_amdgcn_fdot2_f32_bf16(__builtin_bit_cast(bf16x2, k), __builtin_bit_cast(bf16x2, q), acc, false);
}
template <int CTRL> __device__ __forceinline__ float dpp(float x) {
  return __builtin_bit_cast(float, __builtin_amdgcn_mov_dpp(__builtin_bit_cast(int, x), CTRL, 0xf, 0xf, true));
}
constexpr int ROR8 = 0x128, ROR4 = 0x124, ROR2 = 0x122, ROR1 = 0x121;
__device__ __forceinline__ float row_sum16(float x) {
  x += dpp<ROR8>(x); x += dpp<ROR4>(x); x += dpp<ROR2>(x); x += dpp<ROR1>(x);
  return x;
}
__device__ __forceinline__ u32x4 ldnt(const u32x4* p) { return __builtin_nontemporal_load(p); }

constexpr int REC = 132;

__global__ void __launch_bounds__(NTHR) attn_split(const __hip_bfloat16* __restrict__ q, const __hip_bfloat16* __restrict__ k,
                                                   const __hip_bfloat16* __restrict__ v, float* __restrict__ ws) {
  __shared__ __attribute__((aligned(16))) float lds[WAVES * 4 * REC];
  const int head = blockIdx.x / NSPLIT, split = blockIdx.x % NSPLIT;
  const int tid = threadIdx.x, lane = tid & 63, wave = __builtin_amdgcn_readfirstlane(tid >> 6);
  const int r = lane >> 4, c = lane & 15;
  constexpr float C2 = 0.088388347648318440f * 1.4426950408889634f;

  const size_t base = ((size_t)head * SKV + (size_t)split * TOK_PER_SPLIT + (size_t)wave * TILE) * D;
  const u32x4* kp = reinterpret_cast<const u32x4*>(k + base) + lane;
  const u32x4* vp = reinterpret_cast<const u32x4*>(v + base) + lane;
  constexpr int STEP = TILE * WAVES * D / 8;

  u32x4 Kn[U], Vn[U];
#pragma unroll
  for (int i = 0; i < U; ++i) Kn[i] = ldnt(kp + i * 64);
#pragma unroll
  for (int i = 0; i < U; ++i) Vn[i] = ldnt(vp + i * 64);
  const u32x4 qv = *reinterpret_cast<const u32x4*>(q + head * D + c * 8);

  float m = -INFINITY, l = 0.f, acc[8];
#pragma unroll
  for (int j = 0; j < 8; ++j) acc[j] = 0.f;

  for (int t = 0; t < NT; ++t) {
    u32x4 Kc[U], Vc[U];
#pragma unroll
    for (int i = 0; i < U; ++i) { Kc[i] = Kn[i]; Vc[i] = Vn[i]; }
    if (t + 1 < NT) {
      kp += STEP; vp += STEP;
#pragma unroll
      for (int i = 0; i < U; ++i) Kn[i] = ldnt(kp + i * 64);
#pragma unroll
      for (int i = 0; i < U; ++i) Vn[i] = ldnt(vp + i * 64);
    }
    float s[U];
#pragma unroll
    for (int i = 0; i < U; ++i) {
      float d = dot2(Kc[i][0], qv[0], 0.f);
      d = dot2(Kc[i][1], qv[1], d); d = dot2(Kc[i][2], qv[2], d); d = dot2(Kc[i][3], qv[3], d);
      s[i] = row_sum16(d) * C2;
    }
    float mx = m;
#pragma unroll
    for (int i = 0; i < U; ++i) mx = fmaxf(mx, s[i]);
    const float alpha = __builtin_amdgcn_exp2f(m - mx);
    m = mx;
    l *= alpha;
#pragma unroll
    for (int j = 0; j < 8; ++j) acc[j] *= alpha;
#pragma unroll
    for (int i = 0; i < U; ++i) {
      const float p = __builtin_amdgcn_exp2f(s[i] - m);
      l += p;
#pragma unroll
      for (int j = 0; j < 4; ++j) {
        acc[2 * j] = fmaf(p, __builtin_bit_cast(float, Vc[i][j] << 16), acc[2 * j]);
        acc[2 * j + 1] = fmaf(p, __builtin_bit_cast(float, Vc[i][j] & 0xffff0000u), acc[2 * j + 1]);
      }
    }
  }
  float* rec = lds + (wave * 4 + r) * REC;
  *reinterpret_cast<f32x4*>(rec + 8 * c) = f32x4{acc[0], acc[1], acc[2], acc[3]};
  *reinterpret_cast<f32x4*>(rec + 8 * c + 4) = f32x4{acc[4], acc[5], acc[6], acc[7]};
  if (c == 0) { rec[128] = m; rec[129] = l; }
  __syncthreads();
  if (tid < 128) {
    float M = -INFINITY;
#pragma unroll
    for (int w = 0; w < WAVES * 4; ++w) M = fmaxf(M, lds[w * REC + 128]);
    float L = 0.f, o = 0.f;
#pragma unroll
    for (int w = 0; w < WAVES * 4; ++w) {
      const float wt = __builtin_amdgcn_exp2f(lds[w * REC + 128] - M);
      L = fmaf(wt, lds[w * REC + 129], L);
      o = fmaf(wt, lds[w * REC + tid], o);
    }
    float* out = ws + (size_t)blockIdx.x * REC;
    out[tid] = o;
    if (tid == 0) { out[128] = M; out[129] = L; }
  }
}

__global__ void __launch_bounds__(128) attn_combine(const float* __restrict__ ws, __hip_bfloat16* __restrict__ out) {
  const int head = blockIdx.x, tid = threadIdx.x;
  const float* p = ws + (size_t)head * NSPLIT * REC;
  float mm[NSPLIT], ll[NSPLIT], oo[NSPLIT];
#pragma unroll
  for (int s = 0; s < NSPLIT; ++s) { mm[s] = p[s * REC + 128]; ll[s] = p[s * REC + 129]; oo[s] = p[s * REC + tid]; }
  float M = -INFINITY;
#pragma unroll
  for (int s = 0; s < NSPLIT; ++s) M = fmaxf(M, mm[s]);
  float L = 0.f, o = 0.f;
#pragma unroll
  for (int s = 0; s < NSPLIT; ++s) {
    const float wt = __builtin_amdgcn_exp2f(mm[s] - M);
    L = fmaf(wt, ll[s], L);
    o = fmaf(wt, oo[s], o);
  }
  out[head * D + tid] = __float2bfloat16(o / L);
}

extern "C" void kernel_launch(void* const* d_in, const int* in_sizes, int n_in, void* d_out, int out_size, void* d_ws,
                              size_t ws_size, hipStream_t stream) {
  const __hip_bfloat16* q = (const __hip_bfloat16*)d_in[0];
  const __hip_bfloat16* k = (const __hip_bfloat16*)d_in[1];
  const __hip_bfloat16* v = (const __hip_bfloat16*)d_in[2];
  attn_split<<<NH * NSPLIT, NTHR, 0, stream>>>(q, k, v, (float*)d_ws);
  attn_combine<<<NH, 128, 0, stream>>>((const float*)d_ws, (__hip_bfloat16*)d_out);
}
```

```cpp
#define KNT 1
#define VNT 1
#define KU 4
#include <hip/hip_runtime.h>
#include <hip/hip_bf16.h>
#include <cstdint>

#define NSPLIT 8
#define WAVES 4
constexpr int U = KU;

constexpr int D = 128;
constexpr int NH = 64;
constexpr int SKV = 16384;
constexpr int NTHR = WAVES * 64;
constexpr int TOK_PER_SPLIT = SKV / NSPLIT;
constexpr int TILE = 4 * U;
constexpr int NT = TOK_PER_SPLIT / (TILE * WAVES);
static_assert(NT * TILE * WAVES == TOK_PER_SPLIT, "");

using bf16x2 = __attribute__((ext_vector_type(2))) __bf16;
using u32x4 = __attribute__((ext_vector_type(4))) unsigned;
using f32x4 = __attribute__((ext_vector_type(4))) float;

__device__ __forceinline__ float dot2(unsigned k, unsigned q, float acc) {
  return __builtin_amdgcn_fdot2_f32_bf16(__builtin_bit_cast(bf16x2, k), __builtin_bit_cast(bf16x2, q), acc, false);
}
template <int CTRL> __device__ __forceinline__ float dpp(float x) {
  return __builtin_bit_cast(float, __builtin_amdgcn_mov_dpp(__builtin_bit_cast(int, x), CTRL, 0xf, 0xf, true));
}
constexpr int ROR8 = 0x128, ROR4 = 0x124, ROR2 = 0x122, ROR1 = 0x121;
__device__ __forceinline__ float row_sum16(float x) {
  x += dpp<ROR8>(x); x += dpp<ROR4>(x); x += dpp<ROR2>(x); x += dpp<ROR1>(x);
  return x;
}
__device__ __forceinline__ u32x4 ldk(const u32x4* p) { return KNT ? __builtin_nontemporal_load(p) : *p; }
__device__ __forceinline__ u32x4 ldv(const u32x4* p) { return VNT ? __builtin_nontemporal_load(p) : *p; }

constexpr int REC = 132;

__global__ void __launch_bounds__(NTHR) attn_split(const __hip_bfloat16* __restrict__ q, const __hip_bfloat16* __restrict__ k,
                                                   const __hip_bfloat16* __restrict__ v, float* __restrict__ ws) {
  __shared__ __attribute__((aligned(16))) float lds[WAVES * 4 * REC];
  const int head = blockIdx.x / NSPLIT, split = blockIdx.x % NSPLIT;
  const int tid = threadIdx.x, lane = tid & 63, wave = __builtin_amdgcn_readfirstlane(tid >> 6);
  const int r = lane >> 4, c = lane & 15;
  constexpr float C2 = 0.088388347648318440f * 1.4426950408889634f;

  const size_t base = ((size_t)head * SKV + (size_t)split * TOK_PER_SPLIT + (size_t)wave * TILE) * D;
  const u32x4* kp = reinterpret_cast<const u32x4*>(k + base) + lane;
  const u32x4* vp = reinterpret_cast<const u32x4*>(v + base) + lane;
  constexpr int STEP = TILE * WAVES * D / 8;

  u32x4 Kn[U], Vn[U];
#pragma unroll
  for (int i = 0; i < U; ++i) Kn[i] = ldk(kp + i * 64);
#pragma unroll
  for (int i = 0; i < U; ++i) Vn[i] = ldv(vp + i * 64);
  const u32x4 qv = *reinterpret_cast<const u32x4*>(q + head * D + c * 8);

  float m = -INFINITY, l = 0.f, acc[8];
#pragma unroll
  for (int j = 0; j < 8; ++j) acc[j] = 0.f;

  for (int t = 0; t < NT; ++t) {
    u32x4 Kc[U], Vc[U];
#pragma unroll
    for (int i = 0; i < U; ++i) { Kc[i] = Kn[i]; Vc[i] = Vn[i]; }
    if (t + 1 < NT) {
      kp += STEP; vp += STEP;
#pragma unroll
      for (int i = 0; i < U; ++i) Kn[i] = ldk(kp + i * 64);
#pragma unroll
      for (int i = 0; i < U; ++i) Vn[i] = ldv(vp + i * 64);
    }
    float s[U];
#pragma unroll
    for (int i = 0; i < U; ++i) {
      float d = dot2(Kc[i][0], qv[0], 0.f);
      d = dot2(Kc[i][1], qv[1], d); d = dot2(Kc[i][2], qv[2], d); d = dot2(Kc[i][3], qv[3], d);
      s[i] = row_sum16(d) * C2;
    }
    float mx = m;
#pragma unroll
    for (int i = 0; i < U; ++i) mx = fmaxf(mx, s[i]);
    const float alpha = __builtin_amdgcn_exp2f(m - mx);
    m = mx;
    l *= alpha;
#pragma unroll
    for (int j = 0; j < 8; ++j) acc[j] *= alpha;
#pragma unroll
    for (int i = 0; i < U; ++i) {
      const float p = __builtin_amdgcn_exp2f(s[i] - m);
      l += p;
#pragma unroll
      for (int j = 0; j < 4; ++j) {
        acc[2 * j] = fmaf(p, __builtin_bit_cast(float, Vc[i][j] << 16), acc[2 * j]);
        acc[2 * j + 1] = fmaf(p, __builtin_bit_cast(float, Vc[i][j] & 0xffff0000u), acc[2 * j + 1]);
      }
    }
  }
  float* rec = lds + (wave * 4 + r) * REC;
  *reinterpret_cast<f32x4*>(rec + 8 * c) = f32x4{acc[0], acc[1], acc[2], acc[3]};
  *reinterpret_cast<f32x4*>(rec + 8 * c + 4) = f32x4{acc[4], acc[5], acc[6], acc[7]};
  if (c == 0) { rec[128] = m; rec[129] = l; }
  __syncthreads();
  if (tid < 128) {
    float M = -INFINITY;
#pragma unroll
    for (int w = 0; w < WAVES * 4; ++w) M = fmaxf(M, lds[w * REC + 128]);
    float L = 0.f, o = 0.f;
#pragma unroll
    for (int w = 0; w < WAVES * 4; ++w) {
      const float wt = __builtin_amdgcn_exp2f(lds[w * REC + 128] - M);
      L = fmaf(wt, lds[w * REC + 129], L);
      o = fmaf(wt, lds[w * REC + tid], o);
    }
    float* out = ws + (size_t)blockIdx.x * REC;
    out[tid] = o;
    if (tid == 0) { out[128] = M; out[129] = L; }
  }
}

__global__ void __launch_bounds__(128) attn_combine(const float* __restrict__ ws, __hip_bfloat16* __restrict__ out) {
  const int head = blockIdx.x, tid = threadIdx.x;
  const float* p = ws + (size_t)head * NSPLIT * REC;
  float mm[NSPLIT], ll[NSPLIT], oo[NSPLIT];
#pragma unroll
  for (int s = 0; s < NSPLIT; ++s) { mm[s] = p[s * REC + 128]; ll[s] = p[s * REC + 129]; oo[s] = p[s * REC + tid]; }
  float M = -INFINITY;
#pragma unroll
  for (int s = 0; s < NSPLIT; ++s) M = fmaxf(M, mm[s]);
  float L = 0.f, o = 0.f;
#pragma unroll
  for (int s = 0; s < NSPLIT; ++s) {
    const float wt = __builtin_amdgcn_exp2f(mm[s] - M);
    L = fmaf(wt, ll[s], L);
    o = fmaf(wt, oo[s], o);
  }
  out[head * D + tid] = __float2bfloat16(o / L);
}

extern "C" void kernel_launch(void* const* d_in, const int* in_sizes, int n_in, void* d_out, int out_size, void* d_ws,
                              size_t ws_size, hipStream_t stream) {
  const __hip_bfloat16* q = (const __hip_bfloat16*)d_in[0];
  const __hip_bfloat16* k = (const __hip_bfloat16*)d_in[1];
  const __hip_bfloat16* v = (const __hip_bfloat16*)d_in[2];
  attn_split<<<NH * NSPLIT, NTHR, 0, stream>>>(q, k, v, (float*)d_ws);
  attn_combine<<<NH, 128, 0, stream>>>((const float*)d_ws, (__hip_bfloat16*)d_out);
}
```

```cpp
#define PROBE 0
#define KNT 1
#define VNT 1
#define KU 4
#include <hip/hip_runtime.h>
#include <hip/hip_bf16.h>
#include <cstdint>

#define NSPLIT 8
#define WAVES 4
constexpr int U = KU;

constexpr int D = 128;
constexpr int NH = 64;
constexpr int SKV = 16384;
constexpr int NTHR = WAVES * 64;
constexpr int TOK_PER_SPLIT = SKV / NSPLIT;
constexpr int TILE = 4 * U;
constexpr int NT = TOK_PER_SPLIT / (TILE * WAVES);
static_assert(NT * TILE * WAVES == TOK_PER_SPLIT, "");

using bf16x2 = __attribute__((ext_vector_type(2))) __bf16;
using u32x4 = __attribute__((ext_vector_type(4))) unsigned;
using f32x4 = __attribute__((ext_vector_type(4))) float;

__device__ __forceinline__ float dot2(unsigned k, unsigned q, float acc) {
  return __builtin_amdgcn_fdot2_f32_bf16(__builtin_bit_cast(bf16x2, k), __builtin_bit_cast(bf16x2, q), acc, false);
}
template <int CTRL> __device__ __forceinline__ float dpp(float x) {
  return __builtin_bit_cast(float, __builtin_amdgcn_mov_dpp(__builtin_bit_cast(int, x), CTRL, 0xf, 0xf, true));
}
constexpr int ROR8 = 0x128, ROR4 = 0x124, ROR2 = 0x122, ROR1 = 0x121;
__device__ __forceinline__ float row_sum16(float x) {
  x += dpp<ROR8>(x); x += dpp<ROR4>(x); x += dpp<ROR2>(x); x += dpp<ROR1>(x);
  return x;
}
__device__ __forceinline__ u32x4 ldk(const u32x4* p) { return KNT ? __builtin_nontemporal_load(p) : *p; }
__device__ __forceinline__ u32x4 ldv(const u32x4* p) { return VNT ? __builtin_nontemporal_load(p) : *p; }

constexpr int REC = 132;

__global__ void __launch_bounds__(NTHR) attn_split(const __hip_bfloat16* __restrict__ q, const __hip_bfloat16* __restrict__ k,
                                                   const __hip_bfloat16* __restrict__ v, float* __restrict__ ws) {
  __shared__ __attribute__((aligned(16))) float lds[WAVES * 4 * REC];
  unsigned long long t0 = __builtin_amdgcn_s_memrealtime();
  const int head = blockIdx.x / NSPLIT, split = blockIdx.x % NSPLIT;
  const int tid = threadIdx.x, lane = tid & 63, wave = __builtin_amdgcn_readfirstlane(tid >> 6);
  const int r = lane >> 4, c = lane & 15;
  constexpr float C2 = 0.088388347648318440f * 1.4426950408889634f;

  const size_t base = ((size_t)head * SKV + (size_t)split * TOK_PER_SPLIT + (size_t)wave * TILE) * D;
  const u32x4* kp = reinterpret_cast<const u32x4*>(k + base) + lane;
  const u32x4* vp = reinterpret_cast<const u32x4*>(v + base) + lane;
  constexpr int STEP = TILE * WAVES * D / 8;

  u32x4 Kn[U], Vn[U];
#pragma unroll
  for (int i = 0; i < U; ++i) Kn[i] = ldk(kp + i * 64);
#pragma unroll
  for (int i = 0; i < U; ++i) Vn[i] = ldv(vp + i * 64);
  const u32x4 qv = *reinterpret_cast<const u32x4*>(q + head * D + c * 8);

  asm volatile("s_waitcnt vmcnt(%0)" :: "i"(2 * U) : "memory");
  unsigned long long tf = __builtin_amdgcn_s_memrealtime();
  float m = -INFINITY, l = 0.f, acc[8];
#pragma unroll
  for (int j = 0; j < 8; ++j) acc[j] = 0.f;

  for (int t = 0; t < NT; ++t) {
    u32x4 Kc[U], Vc[U];
#pragma unroll
    for (int i = 0; i < U; ++i) { Kc[i] = Kn[i]; Vc[i] = Vn[i]; }
    if (t + 1 < NT) {
      kp += STEP; vp += STEP;
#pragma unroll
      for (int i = 0; i < U; ++i) Kn[i] = ldk(kp + i * 64);
#pragma unroll
      for (int i = 0; i < U; ++i) Vn[i] = ldv(vp + i * 64);
    }
    float s[U];
#pragma unroll
    for (int i = 0; i < U; ++i) {
      float d = dot2(Kc[i][0], qv[0], 0.f);
      d = dot2(Kc[i][1], qv[1], d); d = dot2(Kc[i][2], qv[2], d); d = dot2(Kc[i][3], qv[3], d);
      s[i] = row_sum16(d) * C2;
    }
    float mx = m;
#pragma unroll
    for (int i = 0; i < U; ++i) mx = fmaxf(mx, s[i]);
    const float alpha = __builtin_amdgcn_exp2f(m - mx);
    m = mx;
    l *= alpha;
#pragma unroll
    for (int j = 0; j < 8; ++j) acc[j] *= alpha;
#pragma unroll
    for (int i = 0; i < U; ++i) {
      const float p = __builtin_amdgcn_exp2f(s[i] - m);
      l += p;
#pragma unroll
      for (int j = 0; j < 4; ++j) {
        acc[2 * j] = fmaf(p, __builtin_bit_cast(float, Vc[i][j] << 16), acc[2 * j]);
        acc[2 * j + 1] = fmaf(p, __builtin_bit_cast(float, Vc[i][j] & 0xffff0000u), acc[2 * j + 1]);
      }
    }
  }
  float* rec = lds + (wave * 4 + r) * REC;
  *reinterpret_cast<f32x4*>(rec + 8 * c) = f32x4{acc[0], acc[1], acc[2], acc[3]};
  *reinterpret_cast<f32x4*>(rec + 8 * c + 4) = f32x4{acc[4], acc[5], acc[6], acc[7]};
  if (c == 0) { rec[128] = m; rec[129] = l; }
  __syncthreads();
  if (tid < 128) {
    float M = -INFINITY;
#pragma unroll
    for (int w = 0; w < WAVES * 4; ++w) M = fmaxf(M, lds[w * REC + 128]);
    float L = 0.f, o = 0.f;
#pragma unroll
    for (int w = 0; w < WAVES * 4; ++w) {
      const float wt = __builtin_amdgcn_exp2f(lds[w * REC + 128] - M);
      L = fmaf(wt, lds[w * REC + 129], L);
      o = fmaf(wt, lds[w * REC + tid], o);
    }
    float* out = ws + (size_t)blockIdx.x * REC;
    out[tid] = o;
    if (tid == 0) { out[128] = M; out[129] = L; }
  }
  unsigned long long t1 = __builtin_amdgcn_s_memrealtime();
  unsigned long long* ts = (unsigned long long*)(ws + (1 << 20)) + (blockIdx.x * WAVES + wave) * 4;
  if (lane == 0) { ts[0] = t0; ts[1] = tf; ts[2] = t1; }
}

__global__ void __launch_bounds__(128) attn_combine(float* __restrict__ ws, __hip_bfloat16* __restrict__ out) {
  unsigned long long t0 = __builtin_amdgcn_s_memrealtime();
  const int head = blockIdx.x, tid = threadIdx.x;
  const float* p = ws + (size_t)head * NSPLIT * REC;
  float mm[NSPLIT], ll[NSPLIT], oo[NSPLIT];
#pragma unroll
  for (int s = 0; s < NSPLIT; ++s) { mm[s] = p[s * REC + 128]; ll[s] = p[s * REC + 129]; oo[s] = p[s * REC + tid]; }
  float M = -INFINITY;
#pragma unroll
  for (int s = 0; s < NSPLIT; ++s) M = fmaxf(M, mm[s]);
  float L = 0.f, o = 0.f;
#pragma unroll
  for (int s = 0; s < NSPLIT; ++s) {
    const float wt = __builtin_amdgcn_exp2f(mm[s] - M);
    L = fmaf(wt, ll[s], L);
    o = fmaf(wt, oo[s], o);
  }
  out[head * D + tid] = __float2bfloat16(o / L);
  unsigned long long t1 = __builtin_amdgcn_s_memrealtime();
  unsigned long long* ts = (unsigned long long*)(ws + (2 << 20)) + blockIdx.x * 2;
  if (tid == 0) { ts[0] = t0; ts[1] = t1; }
}
__global__ void __launch_bounds__(64) diag(const float* __restrict__ ws, float* sink) {
  const unsigned long long* ts = (const unsigned long long*)(ws + (1 << 20));
  const unsigned long long* tc = (const unsigned long long*)(ws + (2 << 20));
  const int lane = threadIdx.x;
  unsigned long long mn = ~0ull, mx = 0, ls = 0, mf = 0, sf = 0;
  for (int w = lane; w < NH * NSPLIT * WAVES; w += 64) { unsigned long long a = ts[4 * w], b = ts[4 * w + 1], c = ts[4 * w + 2];
    mn = a < mn ? a : mn; mx = c > mx ? c : mx; ls = a > ls ? a : ls; mf = b > mf ? b : mf; sf += b - a; }
  unsigned long long cs = tc[2 * lane], ce = tc[2 * lane + 1];
  for (int o = 32; o; o >>= 1) {
    unsigned long long x;
    x = __shfl_xor(mn, o); mn = x < mn ? x : mn; x = __shfl_xor(mx, o); mx = x > mx ? x : mx; x = __shfl_xor(ls, o); ls = x > ls ? x : ls;
    x = __shfl_xor(mf, o); mf = x > mf ? x : mf; sf += __shfl_xor(sf, o); x = __shfl_xor(cs, o); cs = x < cs ? x : cs; x = __shfl_xor(ce, o); ce = x > ce ? x : ce; }
  unsigned long long f = 0;
  if (PROBE == 1) f = mx - mn;
  if (PROBE == 2) f = 10 * (mf - mn);
  if (PROBE == 3) f = 10 * sf / (NH * NSPLIT * WAVES);
  if (PROBE == 4) f = 10 * (cs - mx);
  if (PROBE == 5) f = 10 * (ce - cs);
  if (PROBE == 6) f = 10 * (ls - mn);
  if (f > 100000) f = 100000;
  unsigned long long t = __builtin_amdgcn_s_memrealtime();
  while (__builtin_amdgcn_s_memrealtime() - t < f) __builtin_amdgcn_s_sleep(1);
  if (f == 0x7fffffffffffull) sink[lane] = 1.f;
}

extern "C" void kernel_launch(void* const* d_in, const int* in_sizes, int n_in, void* d_out, int out_size, void* d_ws,
                              size_t ws_size, hipStream_t stream) {
  const __hip_bfloat16* q = (const __hip_bfloat16*)d_in[0];
  const __hip_bfloat16* k = (const __hip_bfloat16*)d_in[1];
  const __hip_bfloat16* v = (const __hip_bfloat16*)d_in[2];
  attn_split<<<NH * NSPLIT, NTHR, 0, stream>>>(q, k, v, (float*)d_ws);
  attn_combine<<<NH, 128, 0, stream>>>((float*)d_ws, (__hip_bfloat16*)d_out);
  diag<<<1, 64, 0, stream>>>((const float*)d_ws, (float*)d_ws + (3 << 20));
}
```

```cpp
#define PROBE 0
#define KSR 22
#include <hip/hip_runtime.h>
#include <hip/hip_bf16.h>
#include <cstdint>
#include <cstdio>

#define KU 8
#define KW 4
#define KTMO 50000

constexpr int D = 128, NH = 64, SKV = 16384;
constexpr int U = KU, WAVES = KW, NTHR = WAVES * 64;
constexpr int GW = 8;
constexpr int NGRP = NH / 2, NBLK = NGRP * GW;
constexpr unsigned HB = SKV * D * 2, QB = 2 * HB;
constexpr unsigned TILE = U * 1024, RB = TILE * WAVES;
constexpr unsigned RPG = QB / RB;
constexpr unsigned SR = KSR, NPOOL = RPG - GW * SR;
constexpr unsigned POOL0 = GW * SR * RB;
constexpr unsigned SENT = 0xffffffffu;
static_assert(SR >= 2 && GW * SR <= RPG && HB % RB == 0, "");
constexpr size_t WS_REC = 65536;
static_assert(NGRP * WAVES * 128 <= WS_REC, "");
constexpr int REC = 132;

using bf16x2 = __attribute__((ext_vector_type(2))) __bf16;
using u32x4 = __attribute__((ext_vector_type(4))) unsigned;
using f32x4 = __attribute__((ext_vector_type(4))) float;
using f32x2 = __attribute__((ext_vector_type(2))) float;

__device__ __forceinline__ float dot2(unsigned k, unsigned q, float acc) {
  return __builtin_amdgcn_fdot2_f32_bf16(__builtin_bit_cast(bf16x2, k), __builtin_bit_cast(bf16x2, q), acc, false);
}
template <int CTRL> __device__ __forceinline__ float dpp(float x) {
  return __builtin_bit_cast(float, __builtin_amdgcn_mov_dpp(__builtin_bit_cast(int, x), CTRL, 0xf, 0xf, true));
}
__device__ __forceinline__ float row_sum16(float x) {
  x += dpp<0x128>(x); x += dpp<0x124>(x); x += dpp<0x122>(x); x += dpp<0x121>(x);
  return x;
}
__device__ __forceinline__ unsigned nosent(float x) { const unsigned u = __builtin_bit_cast(unsigned, x); return u == SENT ? 0x7fc00000u : u; }
#define SB() __builtin_amdgcn_sched_barrier(0)

__global__ void __launch_bounds__(NTHR) attn_decode(const char* __restrict__ k, const char* __restrict__ v,
                                                    const __hip_bfloat16* __restrict__ q, char* __restrict__ ws,
                                                    __hip_bfloat16* __restrict__ out) {
  __shared__ __attribute__((aligned(16))) float lds[2 * WAVES * 4 * REC + 2 * WAVES];
  const unsigned long long dt0 = __builtin_amdgcn_s_memrealtime();
  const int tid = threadIdx.x, lane = tid & 63, wave = __builtin_amdgcn_readfirstlane(tid >> 6);
  const int grp = blockIdx.x / GW, g = blockIdx.x % GW;
  const auto rk = __builtin_amdgcn_make_buffer_rsrc((void*)(k + (size_t)grp * QB), 0, (int)QB, 0x00020000);
  const auto rv = __builtin_amdgcn_make_buffer_rsrc((void*)(v + (size_t)grp * QB), 0, (int)QB, 0x00020000);
  const unsigned vo = lane * 16 + wave * TILE;
  unsigned off = g * SR * RB;
  const int r = lane >> 4, c = lane & 15;
  u32x4 qa = *reinterpret_cast<const u32x4*>(q + grp * (2 * D) + c * 8);
  u32x4 qb = *reinterpret_cast<const u32x4*>(q + grp * (2 * D) + D + c * 8);
  SB();
  u32x4 Kr[U], Vr[U];
#pragma unroll
  for (int i = 0; i < U; ++i) Kr[i] = __builtin_amdgcn_raw_buffer_load_b128(rk, vo + i * 1024, off, 2);
#pragma unroll
  for (int i = 0; i < U; ++i) Vr[i] = __builtin_amdgcn_raw_buffer_load_b128(rv, vo + i * 1024, off, 2);
  SB();
  constexpr float C2 = 0.088388347648318440f * 1.4426950408889634f;
  asm volatile("" : "+v"(qa), "+v"(qb));
  const auto rc = __builtin_amdgcn_make_buffer_rsrc((void*)(ws + (grp * WAVES + wave) * 128), 0, 4, 0x00020000);
  const auto rr = __builtin_amdgcn_make_buffer_rsrc((void*)(ws + WS_REC + (size_t)grp * (GW * 2 * D * 16)), 0, GW * 2 * D * 16, 0x00020000);
  const unsigned ro = tid < 2 * D ? tid * 16 : GW * 2 * D * 16;
  if (g == 0) {
    __builtin_amdgcn_raw_buffer_store_b32(0u, rc, lane * 4, 0, 16);
#pragma unroll
    for (int s = 1; s < GW; ++s) __builtin_amdgcn_raw_buffer_store_b128(u32x4{SENT, SENT, SENT, SENT}, rr, ro, s * (2 * D * 16), 16);
  }
  unsigned nstat = SR, npool = NPOOL;

  while (true) {
    unsigned pend = 0, n = 0;
#pragma unroll 1
    for (int ph = 0; ph < 2; ++ph) {
      const u32x4 qv = ph ? qb : qa;
      const unsigned lim = ph ? QB : HB;
      float m = -INFINITY, l = 0.f;
      f32x2 acc[4];
#pragma unroll
      for (int j = 0; j < 4; ++j) acc[j] = f32x2{0.f, 0.f};
#pragma unroll 1
      while (off < lim) {
        ++n;
        const unsigned cl = __builtin_amdgcn_readfirstlane(pend);
        const unsigned dyn = cl < npool ? POOL0 + cl * RB : QB;
        off = n < nstat ? off + RB : dyn;
        pend = __builtin_amdgcn_raw_ptr_buffer_atomic_add_i32(1, rc, lane * 4, (n + 1 < nstat || off >= QB) ? 4 : 0, 0);
        SB();
        float s[U];
#pragma unroll
        for (int i = 0; i < U; ++i) {
          float d = dot2(Kr[i][0], qv[0], 0.f);
          d = dot2(Kr[i][1], qv[1], d); d = dot2(Kr[i][2], qv[2], d); d = dot2(Kr[i][3], qv[3], d);
          Kr[i] = __builtin_amdgcn_raw_buffer_load_b128(rk, vo + i * 1024, off, 2);
          s[i] = d;
          SB();
        }
#pragma unroll
        for (int i = 0; i < U; ++i) s[i] += dpp<0x128>(s[i]);
        SB();
#pragma unroll
        for (int i = 0; i < U; ++i) s[i] += dpp<0x124>(s[i]);
        SB();
#pragma unroll
        for (int i = 0; i < U; ++i) s[i] += dpp<0x122>(s[i]);
        SB();
#pragma unroll
        for (int i = 0; i < U; ++i) s[i] += dpp<0x121>(s[i]);
        float mx = s[0];
#pragma unroll
        for (int i = 1; i < U; ++i) mx = fmaxf(mx, s[i]);
        mx = fmaxf(m, mx * C2);
        const float alpha = __builtin_amdgcn_exp2f(m - mx);
        m = mx;
        l *= alpha;
#pragma unroll
        for (int j = 0; j < 4; ++j) acc[j] *= f32x2{alpha, alpha};
#pragma unroll
        for (int i = 0; i < U; ++i) { s[i] = __builtin_amdgcn_exp2f(fmaf(s[i], C2, -m)); l += s[i]; }
        asm volatile("" : "+v"(acc[0]), "+v"(acc[1]), "+v"(acc[2]), "+v"(acc[3]));
        SB();
#pragma unroll
        for (int i = 0; i < U; ++i) {
#pragma unroll
          for (int j = 0; j < 4; ++j)
            acc[j] = __builtin_elementwise_fma(f32x2{s[i], s[i]},
                                               f32x2{__builtin_bit_cast(float, Vr[i][j] << 16), __builtin_bit_cast(float, Vr[i][j] & 0xffff0000u)}, acc[j]);
          asm volatile("" : "+v"(acc[0]), "+v"(acc[1]), "+v"(acc[2]), "+v"(acc[3]) :: "memory");
          Vr[i] = __builtin_amdgcn_raw_buffer_load_b128(rv, vo + i * 1024, off, 2);
          SB();
        }
      }
      float* rec = lds + ((ph * WAVES + wave) * 4 + r) * REC;
      *reinterpret_cast<f32x4*>(rec + 8 * c) = f32x4{acc[0][0], acc[0][1], acc[1][0], acc[1][1]};
      *reinterpret_cast<f32x4*>(rec + 8 * c + 4) = f32x4{acc[2][0], acc[2][1], acc[3][0], acc[3][1]};
      if (c == 0) { rec[128] = m; rec[129] = l; }
    }
    unsigned* ldsu = reinterpret_cast<unsigned*>(lds + 2 * WAVES * 4 * REC);
    unsigned long long* dts = (unsigned long long*)(ws + (4 << 20)) + (blockIdx.x * WAVES + wave) * 4;
    { const unsigned long long dt1 = __builtin_amdgcn_s_memrealtime(); if (lane == 0) { dts[0] = dt0; dts[1] = dt1; dts[2] = dt1; dts[3] = n; } }
    if (lane == 0) ldsu[wave] = n;
    __syncthreads();
    const int t = tid & (2 * D - 1);
    const float* base = lds + (t >> 7) * (WAVES * 4 * REC);
    float M = -INFINITY, L = 0.f, o = 0.f;
    unsigned cnt = 0;
#pragma unroll
    for (int w = 0; w < WAVES * 4; ++w) M = fmaxf(M, base[w * REC + 128]);
    const float Ms = M == -INFINITY ? 0.f : M;
#pragma unroll
    for (int w = 0; w < WAVES * 4; ++w) {
      const float wt = __builtin_amdgcn_exp2f(base[w * REC + 128] - Ms);
      L = fmaf(wt, base[w * REC + 129], L);
      o = fmaf(wt, base[w * REC + (t & (D - 1))], o);
    }
#pragma unroll
    for (int w = 0; w < WAVES; ++w) cnt += ldsu[w];
    if (g != 0) {
      __builtin_amdgcn_raw_buffer_store_b128(u32x4{nosent(o), nosent(M), nosent(L), cnt}, rr, ro, g * (2 * D * 16), 16);
      return;
    }
    bool ok = true;
    if (nstat == SR) {
      u32x4 p[GW - 1];
      const unsigned long long t0 = __builtin_amdgcn_s_memrealtime();
      do {
        asm volatile("" ::: "memory");
#pragma unroll
        for (int s = 1; s < GW; ++s) p[s - 1] = __builtin_amdgcn_raw_buffer_load_b128(rr, t * 16, s * (2 * D * 16), 16);
        unsigned mxu = 0;
#pragma unroll
        for (int s = 0; s < GW - 1; ++s) mxu = max(max(max(mxu, p[s][0]), max(p[s][1], p[s][2])), p[s][3]);
        ok = __all(mxu != SENT);
      } while (!ok && __builtin_amdgcn_s_memrealtime() - t0 < KTMO);
      float Mx = M;
#pragma unroll
      for (int s = 0; s < GW - 1; ++s) { cnt += p[s][3]; Mx = fmaxf(Mx, __uint_as_float(p[s][1])); }
      ok = ok && __all(cnt == RPG * WAVES);
      const float w0 = __builtin_amdgcn_exp2f(M - Mx);
      L *= w0; o *= w0;
#pragma unroll
      for (int s = 0; s < GW - 1; ++s) {
        const float wt = __builtin_amdgcn_exp2f(__uint_as_float(p[s][1]) - Mx);
        L = fmaf(wt, __uint_as_float(p[s][2]), L);
        o = fmaf(wt, __uint_as_float(p[s][0]), o);
      }
    }
    if (ok && tid < 2 * D) out[grp * 2 * D + tid] = __float2bfloat16(o / L);
    { const unsigned long long dt2 = __builtin_amdgcn_s_memrealtime(); if (lane == 0) dts[2] = dt2; }
    if (lane == 0) ldsu[WAVES + wave] = ok;
    __syncthreads();
    bool all_ok = true;
#pragma unroll
    for (int w = 0; w < WAVES; ++w) all_ok = all_ok && ldsu[WAVES + w];
    if (__builtin_amdgcn_readfirstlane(all_ok)) return;
    off = 0; nstat = RPG + 2; npool = 0;
#pragma unroll
    for (int i = 0; i < U; ++i) Kr[i] = __builtin_amdgcn_raw_buffer_load_b128(rk, vo + i * 1024, off, 2);
#pragma unroll
    for (int i = 0; i < U; ++i) Vr[i] = __builtin_amdgcn_raw_buffer_load_b128(rv, vo + i * 1024, off, 2);
  }
}

__global__ void __launch_bounds__(64) diag(const char* __restrict__ ws, const char* k, const char* v, float* sink) {
  const unsigned long long* ts = (const unsigned long long*)(ws + (4 << 20));
  const int lane = threadIdx.x;
  unsigned long long mn = ~0ull, se = 0, fe = ~0ull, ke = 0, nmin = ~0ull, nmax = 0;
  for (int w = lane; w < NBLK * WAVES; w += 64) { unsigned long long a = ts[4 * w], b = ts[4 * w + 1], c = ts[4 * w + 2], n = ts[4 * w + 3];
    mn = a < mn ? a : mn; se = b > se ? b : se; fe = b < fe ? b : fe; ke = c > ke ? c : ke; nmin = n < nmin ? n : nmin; nmax = n > nmax ? n : nmax; }
  for (int o = 32; o; o >>= 1) {
    unsigned long long x;
    x = __shfl_xor(mn, o); mn = x < mn ? x : mn; x = __shfl_xor(se, o); se = x > se ? x : se; x = __shfl_xor(fe, o); fe = x < fe ? x : fe;
    x = __shfl_xor(ke, o); ke = x > ke ? x : ke; x = __shfl_xor(nmin, o); nmin = x < nmin ? x : nmin; x = __shfl_xor(nmax, o); nmax = x > nmax ? x : nmax; }
  unsigned long long f = (mn + se + fe + ke + nmin + nmax) == 12345 ? 1 : 0;
  if (PROBE == 1) f += ke - mn;
  if (PROBE == 2) f += 10 * (ke - se);
  if (PROBE == 3) f += 10 * (se - fe);
  if (PROBE == 4) f += 300 * nmin;
  if (PROBE == 5) f += (((size_t)k >> 12) & 511) * 40 + (((size_t)k & 4095) ? 30000 : 0);
  if (PROBE == 6) f += (((size_t)v >> 12) & 511) * 40 + (((size_t)v & 4095) ? 30000 : 0);
  if (f > 100000) f = 100000;
  unsigned long long t = __builtin_amdgcn_s_memrealtime();
  while (__builtin_amdgcn_s_memrealtime() - t < f) __builtin_amdgcn_s_sleep(1);
  if (f == 0x7fffffffffffull) sink[lane] = 1.f;
}

extern "C" void kernel_launch(void* const* d_in, const int* in_sizes, int n_in, void* d_out, int out_size, void* d_ws,
                              size_t ws_size, hipStream_t stream) {
  attn_decode<<<NBLK, NTHR, 0, stream>>>((const char*)d_in[1], (const char*)d_in[2], (const __hip_bfloat16*)d_in[0], (char*)d_ws,
                                         (__hip_bfloat16*)d_out);
  diag<<<1, 64, 0, stream>>>((const char*)d_ws, (const char*)d_in[1], (const char*)d_in[2], (float*)d_ws + (3 << 20));
  fprintf(stderr, "ptrs q %p k %p v %p out %p ws %p\n", d_in[0], d_in[1], d_in[2], d_out, d_ws);
}
```

```cpp
#define PROBE 1
#define KSR 22
#include <hip/hip_runtime.h>
#include <hip/hip_bf16.h>
#include <cstdint>

#define KU 8
#define KW 4
#define KTMO 50000

constexpr int D = 128, NH = 64, SKV = 16384;
constexpr int U = KU, WAVES = KW, NTHR = WAVES * 64;
constexpr int GW = 8;
constexpr int NGRP = NH / 2, NBLK = NGRP * GW;
constexpr unsigned HB = SKV * D * 2, QB = 2 * HB;
constexpr unsigned TILE = U * 1024, RB = TILE * WAVES;
constexpr unsigned RPG = QB / RB;
constexpr unsigned SR = KSR, NPOOL = RPG - GW * SR;
constexpr unsigned POOL0 = GW * SR * RB;
constexpr unsigned SENT = 0xffffffffu;
static_assert(SR >= 2 && GW * SR <= RPG && HB % RB == 0, "");
constexpr size_t WS_REC = 65536;
static_assert(NGRP * WAVES * 128 <= WS_REC, "");
constexpr int REC = 132;

using bf16x2 = __attribute__((ext_vector_type(2))) __bf16;
using u32x4 = __attribute__((ext_vector_type(4))) unsigned;
using f32x4 = __attribute__((ext_vector_type(4))) float;
using f32x2 = __attribute__((ext_vector_type(2))) float;

__device__ __forceinline__ float dot2(unsigned k, unsigned q, float acc) {
  return __builtin_amdgcn_fdot2_f32_bf16(__builtin_bit_cast(bf16x2, k), __builtin_bit_cast(bf16x2, q), acc, false);
}
template <int CTRL> __device__ __forceinline__ float dpp(float x) {
  return __builtin_bit_cast(float, __builtin_amdgcn_mov_dpp(__builtin_bit_cast(int, x), CTRL, 0xf, 0xf, true));
}
__device__ __forceinline__ float row_sum16(float x) {
  x += dpp<0x128>(x); x += dpp<0x124>(x); x += dpp<0x122>(x); x += dpp<0x121>(x);
  return x;
}
__device__ __forceinline__ unsigned nosent(float x) { const unsigned u = __builtin_bit_cast(unsigned, x); return u == SENT ? 0x7fc00000u : u; }
#define SB() __builtin_amdgcn_sched_barrier(0)

__global__ void __launch_bounds__(NTHR) attn_decode(const char* __restrict__ k, const char* __restrict__ v,
                                                    const __hip_bfloat16* __restrict__ q, char* __restrict__ ws,
                                                    __hip_bfloat16* __restrict__ out) {
  __shared__ __attribute__((aligned(16))) float lds[2 * WAVES * 4 * REC + 2 * WAVES];
  const int tid = threadIdx.x, lane = tid & 63, wave = __builtin_amdgcn_readfirstlane(tid >> 6);
  const int grp = blockIdx.x / GW, g = blockIdx.x % GW;
  const auto rk = __builtin_amdgcn_make_buffer_rsrc((void*)(k + (size_t)grp * QB), 0, (int)QB, 0x00020000);
  const auto rv = __builtin_amdgcn_make_buffer_rsrc((void*)(v + (size_t)grp * QB), 0, (int)QB, 0x00020000);
  const unsigned vo = lane * 16 + wave * TILE;
  unsigned off = g * SR * RB;
  const int r = lane >> 4, c = lane & 15;
  u32x4 qa = *reinterpret_cast<const u32x4*>(q + grp * (2 * D) + c * 8);
  u32x4 qb = *reinterpret_cast<const u32x4*>(q + grp * (2 * D) + D + c * 8);
  SB();
  u32x4 Kr[U], Vr[U];
#pragma unroll
  for (int i = 0; i < U; ++i) Kr[i] = __builtin_amdgcn_raw_buffer_load_b128(rk, vo + i * 1024, off, 2);
#pragma unroll
  for (int i = 0; i < U; ++i) Vr[i] = __builtin_amdgcn_raw_buffer_load_b128(rv, vo + i * 1024, off, 2);
  SB();
  constexpr float C2 = 0.088388347648318440f * 1.4426950408889634f;
  asm volatile("" : "+v"(qa), "+v"(qb));
  const auto rc = __builtin_amdgcn_make_buffer_rsrc((void*)(ws + (grp * WAVES + wave) * 128), 0, 4, 0x00020000);
  const auto rr = __builtin_amdgcn_make_buffer_rsrc((void*)(ws + WS_REC + (size_t)grp * (GW * 2 * D * 16)), 0, GW * 2 * D * 16, 0x00020000);
  const unsigned ro = tid < 2 * D ? tid * 16 : GW * 2 * D * 16;
  if (g == 0) {
    __builtin_amdgcn_raw_buffer_store_b32(0u, rc, lane * 4, 0, 16);
#pragma unroll
    for (int s = 1; s < GW; ++s) __builtin_amdgcn_raw_buffer_store_b128(u32x4{SENT, SENT, SENT, SENT}, rr, ro, s * (2 * D * 16), 16);
  }
  unsigned nstat = SR, npool = NPOOL;

  while (true) {
    unsigned pend = 0, n = 0;
#pragma unroll 1
    for (int ph = 0; ph < 2; ++ph) {
      const u32x4 qv = ph ? qb : qa;
      const unsigned lim = ph ? QB : HB;
      float m = -INFINITY, l = 0.f;
      f32x2 acc[4];
#pragma unroll
      for (int j = 0; j < 4; ++j) acc[j] = f32x2{0.f, 0.f};
#pragma unroll 1
      while (off < lim) {
        ++n;
        const unsigned cl = __builtin_amdgcn_readfirstlane(pend);
        const unsigned dyn = cl < npool ? POOL0 + cl * RB : QB;
        off = n < nstat ? off + RB : dyn;
        pend = __builtin_amdgcn_raw_ptr_buffer_atomic_add_i32(1, rc, lane * 4, (n + 1 < nstat || off >= QB) ? 4 : 0, 0);
        SB();
        float s[U];
#pragma unroll
        for (int i = 0; i < U; ++i) {
          float d = dot2(Kr[i][0], qv[0], 0.f);
          d = dot2(Kr[i][1], qv[1], d); d = dot2(Kr[i][2], qv[2], d); d = dot2(Kr[i][3], qv[3], d);
          Kr[i] = __builtin_amdgcn_raw_buffer_load_b128(rk, vo + i * 1024, off, 2);
          s[i] = d;
          SB();
        }
#pragma unroll
        for (int i = 0; i < U; ++i) s[i] += dpp<0x128>(s[i]);
        SB();
#pragma unroll
        for (int i = 0; i < U; ++i) s[i] += dpp<0x124>(s[i]);
        SB();
#pragma unroll
        for (int i = 0; i < U; ++i) s[i] += dpp<0x122>(s[i]);
        SB();
#pragma unroll
        for (int i = 0; i < U; ++i) s[i] += dpp<0x121>(s[i]);
        float mx = s[0];
#pragma unroll
        for (int i = 1; i < U; ++i) mx = fmaxf(mx, s[i]);
        mx = fmaxf(m, mx * C2);
        const float alpha = __builtin_amdgcn_exp2f(m - mx);
        m = mx;
        l *= alpha;
#pragma unroll
        for (int j = 0; j < 4; ++j) acc[j] *= f32x2{alpha, alpha};
#pragma unroll
        for (int i = 0; i < U; ++i) { s[i] = __builtin_amdgcn_exp2f(fmaf(s[i], C2, -m)); l += s[i]; }
        asm volatile("" : "+v"(acc[0]), "+v"(acc[1]), "+v"(acc[2]), "+v"(acc[3]));
        SB();
#pragma unroll
        for (int i = 0; i < U; ++i) {
#pragma unroll
          for (int j = 0; j < 4; ++j)
            acc[j] = __builtin_elementwise_fma(f32x2{s[i], s[i]},
                                               f32x2{__builtin_bit_cast(float, Vr[i][j] << 16), __builtin_bit_cast(float, Vr[i][j] & 0xffff0000u)}, acc[j]);
          asm volatile("" : "+v"(acc[0]), "+v"(acc[1]), "+v"(acc[2]), "+v"(acc[3]) :: "memory");
          Vr[i] = __builtin_amdgcn_raw_buffer_load_b128(rv, vo + i * 1024, off, 2);
          SB();
        }
      }
      float* rec = lds + ((ph * WAVES + wave) * 4 + r) * REC;
      *reinterpret_cast<f32x4*>(rec + 8 * c) = f32x4{acc[0][0], acc[0][1], acc[1][0], acc[1][1]};
      *reinterpret_cast<f32x4*>(rec + 8 * c + 4) = f32x4{acc[2][0], acc[2][1], acc[3][0], acc[3][1]};
      if (c == 0) { rec[128] = m; rec[129] = l; }
    }
    unsigned* ldsu = reinterpret_cast<unsigned*>(lds + 2 * WAVES * 4 * REC);
    if (lane == 0) ldsu[wave] = n;
    __syncthreads();
    const int t = tid & (2 * D - 1);
    const float* base = lds + (t >> 7) * (WAVES * 4 * REC);
    float M = -INFINITY, L = 0.f, o = 0.f;
    unsigned cnt = 0;
#pragma unroll
    for (int w = 0; w < WAVES * 4; ++w) M = fmaxf(M, base[w * REC + 128]);
    const float Ms = M == -INFINITY ? 0.f : M;
#pragma unroll
    for (int w = 0; w < WAVES * 4; ++w) {
      const float wt = __builtin_amdgcn_exp2f(base[w * REC + 128] - Ms);
      L = fmaf(wt, base[w * REC + 129], L);
      o = fmaf(wt, base[w * REC + (t & (D - 1))], o);
    }
#pragma unroll
    for (int w = 0; w < WAVES; ++w) cnt += ldsu[w];
    if (g != 0) {
      __builtin_amdgcn_raw_buffer_store_b128(u32x4{nosent(o), nosent(M), nosent(L), cnt}, rr, ro, g * (2 * D * 16), 16);
      return;
    }
    bool ok = true;
    if (nstat == SR) {
      u32x4 p[GW - 1];
      const unsigned long long t0 = __builtin_amdgcn_s_memrealtime();
      do {
        asm volatile("" ::: "memory");
#pragma unroll
        for (int s = 1; s < GW; ++s) p[s - 1] = __builtin_amdgcn_raw_buffer_load_b128(rr, t * 16, s * (2 * D * 16), 16);
        unsigned mxu = 0;
#pragma unroll
        for (int s = 0; s < GW - 1; ++s) mxu = max(max(max(mxu, p[s][0]), max(p[s][1], p[s][2])), p[s][3]);
        ok = __all(mxu != SENT);
      } while (!ok && __builtin_amdgcn_s_memrealtime() - t0 < KTMO);
      float Mx = M;
#pragma unroll
      for (int s = 0; s < GW - 1; ++s) { cnt += p[s][3]; Mx = fmaxf(Mx, __uint_as_float(p[s][1])); }
      ok = ok && __all(cnt == RPG * WAVES);
      const float w0 = __builtin_amdgcn_exp2f(M - Mx);
      L *= w0; o *= w0;
#pragma unroll
      for (int s = 0; s < GW - 1; ++s) {
        const float wt = __builtin_amdgcn_exp2f(__uint_as_float(p[s][1]) - Mx);
        L = fmaf(wt, __uint_as_float(p[s][2]), L);
        o = fmaf(wt, __uint_as_float(p[s][0]), o);
      }
    }
    if (ok && tid < 2 * D) out[grp * 2 * D + tid] = __float2bfloat16(o / L);
    if (lane == 0) ldsu[WAVES + wave] = ok;
    __syncthreads();
    bool all_ok = true;
#pragma unroll
    for (int w = 0; w < WAVES; ++w) all_ok = all_ok && ldsu[WAVES + w];
    if (__builtin_amdgcn_readfirstlane(all_ok)) return;
    off = 0; nstat = RPG + 2; npool = 0;
#pragma unroll
    for (int i = 0; i < U; ++i) Kr[i] = __builtin_amdgcn_raw_buffer_load_b128(rk, vo + i * 1024, off, 2);
#pragma unroll
    for (int i = 0; i < U; ++i) Vr[i] = __builtin_amdgcn_raw_buffer_load_b128(rv, vo + i * 1024, off, 2);
  }
}

__device__ __forceinline__ unsigned lat1(const char* p, unsigned& sink) {
  unsigned long long t0, t1; unsigned x;
  asm volatile("s_memtime %0\n\ts_waitcnt lgkmcnt(0)\n\tglobal_load_dword %2, %3, off nt\n\ts_waitcnt vmcnt(0)\n\ts_memtime %1\n\ts_waitcnt lgkmcnt(0)"
               : "=&s"(t0), "=&s"(t1), "=&v"(x) : "v"(p) : "memory");
  sink += x;
  return (unsigned)(t1 - t0);
}
__global__ void __launch_bounds__(64) diag(const char* k, const char* v, float* sinkp) {
  if (threadIdx.x != 0) return;
  unsigned sink = 0, same = 0, kalt = 0;
  const unsigned long long ts = __builtin_amdgcn_s_memrealtime();
  for (int i = 0; i < 16; ++i) {
    const size_t pg = (size_t)(i * 8 + (i & 1)) << 21;
    lat1(k + pg + 4096 * 4, sink); lat1(v + pg + 4096 * 4, sink);
    unsigned k0 = min(lat1(k + pg, sink), lat1(k + pg + 256, sink)), k1 = min(lat1(k + pg + 8192, sink), lat1(k + pg + 8192 + 256, sink));
    unsigned v0 = min(lat1(v + pg, sink), lat1(v + pg + 256, sink)), v1 = min(lat1(v + pg + 8192, sink), lat1(v + pg + 8192 + 256, sink));
    const unsigned pk = k0 > k1, pv = v0 > v1;
    same += pk == pv;
    kalt += pk ^ (i & 1);
  }
  unsigned long long f = 10000;
  if (PROBE == 1) f += same * 1000;
  if (PROBE == 2) f += kalt * 1000;
  while (__builtin_amdgcn_s_memrealtime() - ts < f) __builtin_amdgcn_s_sleep(1);
  if (sink == 0x7654321) sinkp[0] = 1.f;
}

extern "C" void kernel_launch(void* const* d_in, const int* in_sizes, int n_in, void* d_out, int out_size, void* d_ws,
                              size_t ws_size, hipStream_t stream) {
  attn_decode<<<NBLK, NTHR, 0, stream>>>((const char*)d_in[1], (const char*)d_in[2], (const __hip_bfloat16*)d_in[0], (char*)d_ws,
                                         (__hip_bfloat16*)d_out);
  diag<<<1, 64, 0, stream>>>((const char*)d_in[1], (const char*)d_in[2], (float*)d_ws + (3 << 20));
}
```

```cpp
#define PRE 0
#define POST 0
#define DYNLDS 0
#define BIGV 1
#define SPINALL 1
#define KAUX 2
#define OUTWT 0
#define KSR 22
#include <hip/hip_runtime.h>
#include <hip/hip_bf16.h>
#include <cstdint>

#define KU 8
#define KW 4
#define KTMO 50000

constexpr int D = 128, NH = 64, SKV = 16384;
constexpr int U = KU, WAVES = KW, NTHR = WAVES * 64;
constexpr int GW = 8;
constexpr int NGRP = NH / 2, NBLK = NGRP * GW;
constexpr unsigned HB = SKV * D * 2, QB = 2 * HB;
constexpr unsigned TILE = U * 1024, RB = TILE * WAVES;
constexpr unsigned RPG = QB / RB;
constexpr unsigned SR = KSR, NPOOL = RPG - GW * SR;
constexpr unsigned POOL0 = GW * SR * RB;
constexpr unsigned SENT = 0xffffffffu;
static_assert(SR >= 2 && GW * SR <= RPG && HB % RB == 0, "");
constexpr size_t WS_REC = 65536;
static_assert(NGRP * WAVES * 128 <= WS_REC, "");
constexpr int REC = 132;

using bf16x2 = __attribute__((ext_vector_type(2))) __bf16;
using u32x4 = __attribute__((ext_vector_type(4))) unsigned;
using f32x4 = __attribute__((ext_vector_type(4))) float;
using f32x2 = __attribute__((ext_vector_type(2))) float;

__device__ __forceinline__ float dot2(unsigned k, unsigned q, float acc) {
  return __builtin_amdgcn_fdot2_f32_bf16(__builtin_bit_cast(bf16x2, k), __builtin_bit_cast(bf16x2, q), acc, false);
}
template <int CTRL> __device__ __forceinline__ float dpp(float x) {
  return __builtin_bit_cast(float, __builtin_amdgcn_mov_dpp(__builtin_bit_cast(int, x), CTRL, 0xf, 0xf, true));
}
__device__ __forceinline__ float row_sum16(float x) {
  x += dpp<0x128>(x); x += dpp<0x124>(x); x += dpp<0x122>(x); x += dpp<0x121>(x);
  return x;
}
__device__ __forceinline__ unsigned nosent(float x) { const unsigned u = __builtin_bit_cast(unsigned, x); return u == SENT ? 0x7fc00000u : u; }
#define SB() __builtin_amdgcn_sched_barrier(0)

__global__ void __launch_bounds__(NTHR) attn_decode(const char* __restrict__ k, const char* __restrict__ v,
                                                    const __hip_bfloat16* __restrict__ q, char* __restrict__ ws,
                                                    __hip_bfloat16* __restrict__ out) {
  __shared__ __attribute__((aligned(16))) float lds[2 * WAVES * 4 * REC + 2 * WAVES];
  unsigned long long dt0;
  asm volatile("s_memrealtime %0\n\ts_waitcnt lgkmcnt(0)" : "=s"(dt0) :: "memory");
  if (BIGV) asm volatile("" ::: "v250");
  SB();
  const int tid = threadIdx.x, lane = tid & 63, wave = __builtin_amdgcn_readfirstlane(tid >> 6);
  const int grp = blockIdx.x / GW, g = blockIdx.x % GW;
  const auto rk = __builtin_amdgcn_make_buffer_rsrc((void*)(k + (size_t)grp * QB), 0, (int)QB, 0x00020000);
  const auto rv = __builtin_amdgcn_make_buffer_rsrc((void*)(v + (size_t)grp * QB), 0, (int)QB, 0x00020000);
  const unsigned vo = lane * 16 + wave * TILE;
  unsigned off = g * SR * RB;
  const int r = lane >> 4, c = lane & 15;
  u32x4 qa = *reinterpret_cast<const u32x4*>(q + grp * (2 * D) + c * 8);
  u32x4 qb = *reinterpret_cast<const u32x4*>(q + grp * (2 * D) + D + c * 8);
  SB();
  u32x4 Kr[U], Vr[U];
#pragma unroll
  for (int i = 0; i < U; ++i) Kr[i] = __builtin_amdgcn_raw_buffer_load_b128(rk, vo + i * 1024, off, KAUX);
#pragma unroll
  for (int i = 0; i < U; ++i) Vr[i] = __builtin_amdgcn_raw_buffer_load_b128(rv, vo + i * 1024, off, KAUX);
  SB();
  constexpr float C2 = 0.088388347648318440f * 1.4426950408889634f;
  asm volatile("" : "+v"(qa), "+v"(qb));
  const auto rc = __builtin_amdgcn_make_buffer_rsrc((void*)(ws + (grp * WAVES + wave) * 128), 0, 4, 0x00020000);
  const auto rr = __builtin_amdgcn_make_buffer_rsrc((void*)(ws + WS_REC + (size_t)grp * (GW * 2 * D * 16)), 0, GW * 2 * D * 16, 0x00020000);
  const unsigned ro = tid < 2 * D ? tid * 16 : GW * 2 * D * 16;
  if (g == 0) {
    __builtin_amdgcn_raw_buffer_store_b32(0u, rc, lane * 4, 0, 16);
#pragma unroll
    for (int s = 1; s < GW; ++s) __builtin_amdgcn_raw_buffer_store_b128(u32x4{SENT, SENT, SENT, SENT}, rr, ro, s * (2 * D * 16), 16);
  }
  unsigned nstat = SR, npool = NPOOL;

  while (true) {
    unsigned pend = 0, n = 0;
#pragma unroll 1
    for (int ph = 0; ph < 2; ++ph) {
      const u32x4 qv = ph ? qb : qa;
      const unsigned lim = ph ? QB : HB;
      float m = -INFINITY, l = 0.f;
      f32x2 acc[4];
#pragma unroll
      for (int j = 0; j < 4; ++j) acc[j] = f32x2{0.f, 0.f};
#pragma unroll 1
      while (off < lim) {
        ++n;
        const unsigned cl = __builtin_amdgcn_readfirstlane(pend);
        const unsigned dyn = cl < npool ? POOL0 + cl * RB : QB;
        off = n < nstat ? off + RB : dyn;
        pend = __builtin_amdgcn_raw_ptr_buffer_atomic_add_i32(1, rc, lane * 4, (n + 1 < nstat || off >= QB) ? 4 : 0, 0);
        SB();
        float s[U];
#pragma unroll
        for (int i = 0; i < U; ++i) {
          float d = dot2(Kr[i][0], qv[0], 0.f);
          d = dot2(Kr[i][1], qv[1], d); d = dot2(Kr[i][2], qv[2], d); d = dot2(Kr[i][3], qv[3], d);
          Kr[i] = __builtin_amdgcn_raw_buffer_load_b128(rk, vo + i * 1024, off, KAUX);
          s[i] = d;
          SB();
        }
#pragma unroll
        for (int i = 0; i < U; ++i) s[i] += dpp<0x128>(s[i]);
        SB();
#pragma unroll
        for (int i = 0; i < U; ++i) s[i] += dpp<0x124>(s[i]);
        SB();
#pragma unroll
        for (int i = 0; i < U; ++i) s[i] += dpp<0x122>(s[i]);
        SB();
#pragma unroll
        for (int i = 0; i < U; ++i) s[i] += dpp<0x121>(s[i]);
        float mx = s[0];
#pragma unroll
        for (int i = 1; i < U; ++i) mx = fmaxf(mx, s[i]);
        mx = fmaxf(m, mx * C2);
        const float alpha = __builtin_amdgcn_exp2f(m - mx);
        m = mx;
        l *= alpha;
#pragma unroll
        for (int j = 0; j < 4; ++j) acc[j] *= f32x2{alpha, alpha};
#pragma unroll
        for (int i = 0; i < U; ++i) { s[i] = __builtin_amdgcn_exp2f(fmaf(s[i], C2, -m)); l += s[i]; }
        asm volatile("" : "+v"(acc[0]), "+v"(acc[1]), "+v"(acc[2]), "+v"(acc[3]));
        SB();
#pragma unroll
        for (int i = 0; i < U; ++i) {
#pragma unroll
          for (int j = 0; j < 4; ++j)
            acc[j] = __builtin_elementwise_fma(f32x2{s[i], s[i]},
                                               f32x2{__builtin_bit_cast(float, Vr[i][j] << 16), __builtin_bit_cast(float, Vr[i][j] & 0xffff0000u)}, acc[j]);
          asm volatile("" : "+v"(acc[0]), "+v"(acc[1]), "+v"(acc[2]), "+v"(acc[3]) :: "memory");
          Vr[i] = __builtin_amdgcn_raw_buffer_load_b128(rv, vo + i * 1024, off, KAUX);
          SB();
        }
      }
      float* rec = lds + ((ph * WAVES + wave) * 4 + r) * REC;
      *reinterpret_cast<f32x4*>(rec + 8 * c) = f32x4{acc[0][0], acc[0][1], acc[1][0], acc[1][1]};
      *reinterpret_cast<f32x4*>(rec + 8 * c + 4) = f32x4{acc[2][0], acc[2][1], acc[3][0], acc[3][1]};
      if (c == 0) { rec[128] = m; rec[129] = l; }
    }
    unsigned* ldsu = reinterpret_cast<unsigned*>(lds + 2 * WAVES * 4 * REC);
    if (lane == 0) ldsu[wave] = n;
    __syncthreads();
    const int t = tid & (2 * D - 1);
    const float* base = lds + (t >> 7) * (WAVES * 4 * REC);
    float M = -INFINITY, L = 0.f, o = 0.f;
    unsigned cnt = 0;
#pragma unroll
    for (int w = 0; w < WAVES * 4; ++w) M = fmaxf(M, base[w * REC + 128]);
    const float Ms = M == -INFINITY ? 0.f : M;
#pragma unroll
    for (int w = 0; w < WAVES * 4; ++w) {
      const float wt = __builtin_amdgcn_exp2f(base[w * REC + 128] - Ms);
      L = fmaf(wt, base[w * REC + 129], L);
      o = fmaf(wt, base[w * REC + (t & (D - 1))], o);
    }
#pragma unroll
    for (int w = 0; w < WAVES; ++w) cnt += ldsu[w];
    if (g != 0) {
      __builtin_amdgcn_raw_buffer_store_b128(u32x4{nosent(o), nosent(M), nosent(L), cnt}, rr, ro, g * (2 * D * 16), 16);
      if (SPINALL) { while (__builtin_amdgcn_s_memrealtime() - dt0 < 15000) __builtin_amdgcn_s_sleep(1); }
      return;
    }
    bool ok = true;
    if (nstat == SR) {
      u32x4 p[GW - 1];
      const unsigned long long t0 = __builtin_amdgcn_s_memrealtime();
      do {
        asm volatile("" ::: "memory");
#pragma unroll
        for (int s = 1; s < GW; ++s) p[s - 1] = __builtin_amdgcn_raw_buffer_load_b128(rr, t * 16, s * (2 * D * 16), 16);
        unsigned mxu = 0;
#pragma unroll
        for (int s = 0; s < GW - 1; ++s) mxu = max(max(max(mxu, p[s][0]), max(p[s][1], p[s][2])), p[s][3]);
        ok = __all(mxu != SENT);
      } while (!ok && __builtin_amdgcn_s_memrealtime() - t0 < KTMO);
      float Mx = M;
#pragma unroll
      for (int s = 0; s < GW - 1; ++s) { cnt += p[s][3]; Mx = fmaxf(Mx, __uint_as_float(p[s][1])); }
      ok = ok && __all(cnt == RPG * WAVES);
      const float w0 = __builtin_amdgcn_exp2f(M - Mx);
      L *= w0; o *= w0;
#pragma unroll
      for (int s = 0; s < GW - 1; ++s) {
        const float wt = __builtin_amdgcn_exp2f(__uint_as_float(p[s][1]) - Mx);
        L = fmaf(wt, __uint_as_float(p[s][2]), L);
        o = fmaf(wt, __uint_as_float(p[s][0]), o);
      }
    }
    if (ok && tid < 2 * D) {
      if (OUTWT) { __hip_bfloat16 b = __float2bfloat16(o / L); __hip_atomic_store((unsigned short*)(out + grp * 2 * D + tid), *(unsigned short*)&b, __ATOMIC_RELAXED, __HIP_MEMORY_SCOPE_SYSTEM); }
      else out[grp * 2 * D + tid] = __float2bfloat16(o / L);
    }
    if (lane == 0) ldsu[WAVES + wave] = ok;
    __syncthreads();
    bool all_ok = true;
#pragma unroll
    for (int w = 0; w < WAVES; ++w) all_ok = all_ok && ldsu[WAVES + w];
    if (__builtin_amdgcn_readfirstlane(all_ok)) { { while (__builtin_amdgcn_s_memrealtime() - dt0 < 15000) __builtin_amdgcn_s_sleep(1); } return; }
    off = 0; nstat = RPG + 2; npool = 0;
#pragma unroll
    for (int i = 0; i < U; ++i) Kr[i] = __builtin_amdgcn_raw_buffer_load_b128(rk, vo + i * 1024, off, KAUX);
#pragma unroll
    for (int i = 0; i < U; ++i) Vr[i] = __builtin_amdgcn_raw_buffer_load_b128(rv, vo + i * 1024, off, KAUX);
  }
}

__global__ void tiny(char* ws) { if (ws == nullptr) __builtin_trap(); }

extern "C" void kernel_launch(void* const* d_in, const int* in_sizes, int n_in, void* d_out, int out_size, void* d_ws,
                              size_t ws_size, hipStream_t stream) {
  if (PRE) tiny<<<PRE, 64, 0, stream>>>((char*)d_ws);
  attn_decode<<<NBLK, NTHR, DYNLDS, stream>>>((const char*)d_in[1], (const char*)d_in[2], (const __hip_bfloat16*)d_in[0], (char*)d_ws,
                                         (__hip_bfloat16*)d_out);
  if (POST) tiny<<<1, 64, 0, stream>>>((char*)d_ws);
}
```

```cpp
#define PROBE 0
#define CODEPF 0
#define KSR 20
#include <hip/hip_runtime.h>
#include <hip/hip_bf16.h>
#include <cstdint>

#define KU 8
#define KW 4
#define KLA 1
#define KTMO 50000

constexpr int D = 128, NH = 64, SKV = 16384;
constexpr int U = KU, WAVES = KW, NTHR = WAVES * 64 + 64;
constexpr int GW = 8;
constexpr int NGRP = NH / 2, NBLK = NGRP * GW;
constexpr unsigned HB = SKV * D * 2, QB = 2 * HB;
constexpr unsigned TILE = U * 1024, RB = TILE * WAVES;
constexpr unsigned RPG = QB / RB;
constexpr unsigned SR = KSR, NPOOL = RPG - GW * SR;
constexpr unsigned POOL0 = GW * SR * RB;
constexpr unsigned SENT = 0xffffffffu;
static_assert(SR >= 2 && GW * SR <= RPG && HB % RB == 0, "");
constexpr size_t WS_REC = 65536;
static_assert(NGRP * 256 <= WS_REC, "");
constexpr int REC = 132;

using bf16x2 = __attribute__((ext_vector_type(2))) __bf16;
using u32x4 = __attribute__((ext_vector_type(4))) unsigned;
using f32x4 = __attribute__((ext_vector_type(4))) float;
using f32x2 = __attribute__((ext_vector_type(2))) float;

__device__ __forceinline__ float dot2(unsigned k, unsigned q, float acc) {
  return __builtin_amdgcn_fdot2_f32_bf16(__builtin_bit_cast(bf16x2, k), __builtin_bit_cast(bf16x2, q), acc, false);
}
template <int CTRL> __device__ __forceinline__ float dpp(float x) {
  return __builtin_bit_cast(float, __builtin_amdgcn_mov_dpp(__builtin_bit_cast(int, x), CTRL, 0xf, 0xf, true));
}
__device__ __forceinline__ float row_sum16(float x) {
  x += dpp<0x128>(x); x += dpp<0x124>(x); x += dpp<0x122>(x); x += dpp<0x121>(x);
  return x;
}
__device__ __forceinline__ unsigned nosent(float x) { const unsigned u = __builtin_bit_cast(unsigned, x); return u == SENT ? 0x7fc00000u : u; }
#define SB() __builtin_amdgcn_sched_barrier(0)

__global__ void __launch_bounds__(NTHR) attn_decode(const char* __restrict__ k, const char* __restrict__ v,
                                                    const __hip_bfloat16* __restrict__ q, char* __restrict__ ws,
                                                    __hip_bfloat16* __restrict__ out) {
  __shared__ __attribute__((aligned(16))) float lds[2 * WAVES * 4 * REC + 2 * WAVES + 1 + NPOOL + 1];
  unsigned* const ldsu = reinterpret_cast<unsigned*>(lds + 2 * WAVES * 4 * REC);
  unsigned* const want = ldsu + 2 * WAVES;
  unsigned* const list = want + 1;
  unsigned long long dt0, dta = 0, dtb = 0;
  asm volatile("s_memrealtime %0\n\ts_waitcnt lgkmcnt(0)" : "=s"(dt0) :: "memory");
  SB();
  const int tid = threadIdx.x, lane = tid & 63, wave = __builtin_amdgcn_readfirstlane(tid >> 6);
  const int grp = blockIdx.x / GW, g = blockIdx.x % GW;
  if (wave == WAVES) {
    if (CODEPF) {
      unsigned long long pc; asm volatile("s_getpc_b64 %0" : "=s"(pc));
      const char* cb = (const char*)(pc & ~255ull) - 8192 + lane * 128;
      unsigned x0 = __builtin_nontemporal_load((const unsigned*)cb), x1 = *(const volatile unsigned*)(cb + 8192);
      x0 = *(const volatile unsigned*)cb;
      asm volatile("" :: "v"(x0), "v"(x1));
    }
    for (int i = lane; i < NPOOL + 2; i += 64) want[i] = i ? SENT : 0u;
    const auto rc = __builtin_amdgcn_make_buffer_rsrc((void*)(ws + grp * 256), 0, 4, 0x00020000);
    if (g == 0) __builtin_amdgcn_raw_buffer_store_b32(0u, rc, lane * 4, 0, 16);
    __syncthreads();
    asm volatile("s_waitcnt vmcnt(0)" ::: "memory");
    for (unsigned kk = 0;; ++kk) {
      unsigned cl = NPOOL;
      if (kk < NPOOL) {
        while (__hip_atomic_load(want, __ATOMIC_RELAXED, __HIP_MEMORY_SCOPE_WORKGROUP) <= kk) __builtin_amdgcn_s_sleep(2);
        cl = __builtin_amdgcn_readfirstlane(__builtin_amdgcn_raw_ptr_buffer_atomic_add_i32(1, rc, lane * 4, 0, 0));
      }
      if (lane == 0) __hip_atomic_store(list + kk, cl < NPOOL ? POOL0 + cl * RB : QB, __ATOMIC_RELAXED, __HIP_MEMORY_SCOPE_WORKGROUP);
      if (cl >= NPOOL) return;
    }
  }
  const auto rk = __builtin_amdgcn_make_buffer_rsrc((void*)(k + (size_t)grp * QB), 0, (int)QB, 0x00020000);
  const auto rv = __builtin_amdgcn_make_buffer_rsrc((void*)(v + (size_t)grp * QB), 0, (int)QB, 0x00020000);
  const unsigned vo = lane * 16 + wave * TILE;
  unsigned off = g * SR * RB;
  const int r = lane >> 4, c = lane & 15;
  u32x4 qa = *reinterpret_cast<const u32x4*>(q + grp * (2 * D) + c * 8);
  u32x4 qb = *reinterpret_cast<const u32x4*>(q + grp * (2 * D) + D + c * 8);
  SB();
  u32x4 Kr[U], Vr[U];
#pragma unroll
  for (int i = 0; i < U; ++i) Kr[i] = __builtin_amdgcn_raw_buffer_load_b128(rk, vo + i * 1024, off, 2);
#pragma unroll
  for (int i = 0; i < U; ++i) Vr[i] = __builtin_amdgcn_raw_buffer_load_b128(rv, vo + i * 1024, off, 2);
  SB();
  constexpr float C2 = 0.088388347648318440f * 1.4426950408889634f;
  asm volatile("" : "+v"(qa), "+v"(qb));
  const auto rr = __builtin_amdgcn_make_buffer_rsrc((void*)(ws + WS_REC + (size_t)grp * (GW * 2 * D * 16)), 0, GW * 2 * D * 16, 0x00020000);
  const unsigned ro = tid < 2 * D ? tid * 16 : GW * 2 * D * 16;
  if (g == 0) {
#pragma unroll
    for (int s = 1; s < GW; ++s) __builtin_amdgcn_raw_buffer_store_b128(u32x4{SENT, SENT, SENT, SENT}, rr, ro, s * (2 * D * 16), 16);
  }
  unsigned nstat = SR;
  __builtin_amdgcn_s_barrier();

  while (true) {
    unsigned n = 0;
#pragma unroll 1
    for (int ph = 0; ph < 2; ++ph) {
      const u32x4 qv = ph ? qb : qa;
      const unsigned lim = ph ? QB : HB;
      float m = -INFINITY, l = 0.f;
      f32x2 acc[4];
#pragma unroll
      for (int j = 0; j < 4; ++j) acc[j] = f32x2{0.f, 0.f};
#pragma unroll 1
      while (off < lim) {
        ++n;
        if (n == 2) dta = __builtin_amdgcn_s_memrealtime();
        if (n == 6) dtb = __builtin_amdgcn_s_memrealtime();
        if (n + KLA >= nstat && lane == 0) __hip_atomic_fetch_max(want, n + KLA + 1 - nstat, __ATOMIC_RELAXED, __HIP_MEMORY_SCOPE_WORKGROUP);
        if (n < nstat) off += RB;
        else do off = __builtin_amdgcn_readfirstlane(__hip_atomic_load(list + (n - nstat), __ATOMIC_RELAXED, __HIP_MEMORY_SCOPE_WORKGROUP)); while (off == SENT);
        SB();
        float s[U];
#pragma unroll
        for (int i = 0; i < U; ++i) {
          float d = dot2(Kr[i][0], qv[0], 0.f);
          d = dot2(Kr[i][1], qv[1], d); d = dot2(Kr[i][2], qv[2], d); d = dot2(Kr[i][3], qv[3], d);
          Kr[i] = __builtin_amdgcn_raw_buffer_load_b128(rk, vo + i * 1024, off, 2);
          s[i] = d;
          SB();
        }
#pragma unroll
        for (int i = 0; i < U; ++i) s[i] += dpp<0x128>(s[i]);
        SB();
#pragma unroll
        for (int i = 0; i < U; ++i) s[i] += dpp<0x124>(s[i]);
        SB();
#pragma unroll
        for (int i = 0; i < U; ++i) s[i] += dpp<0x122>(s[i]);
        SB();
#pragma unroll
        for (int i = 0; i < U; ++i) s[i] += dpp<0x121>(s[i]);
        float mx = s[0];
#pragma unroll
        for (int i = 1; i < U; ++i) mx = fmaxf(mx, s[i]);
        mx = fmaxf(m, mx * C2);
        const float alpha = __builtin_amdgcn_exp2f(m - mx);
        m = mx;
        l *= alpha;
#pragma unroll
        for (int j = 0; j < 4; ++j) acc[j] *= f32x2{alpha, alpha};
#pragma unroll
        for (int i = 0; i < U; ++i) { s[i] = __builtin_amdgcn_exp2f(fmaf(s[i], C2, -m)); l += s[i]; }
        asm volatile("" : "+v"(acc[0]), "+v"(acc[1]), "+v"(acc[2]), "+v"(acc[3]));
        SB();
#pragma unroll
        for (int i = 0; i < U; ++i) {
#pragma unroll
          for (int j = 0; j < 4; ++j)
            acc[j] = __builtin_elementwise_fma(f32x2{s[i], s[i]},
                                               f32x2{__builtin_bit_cast(float, Vr[i][j] << 16), __builtin_bit_cast(float, Vr[i][j] & 0xffff0000u)}, acc[j]);
          asm volatile("" : "+v"(acc[0]), "+v"(acc[1]), "+v"(acc[2]), "+v"(acc[3]) :: "memory");
          Vr[i] = __builtin_amdgcn_raw_buffer_load_b128(rv, vo + i * 1024, off, 2);
          SB();
        }
      }
      float* rec = lds + ((ph * WAVES + wave) * 4 + r) * REC;
      *reinterpret_cast<f32x4*>(rec + 8 * c) = f32x4{acc[0][0], acc[0][1], acc[1][0], acc[1][1]};
      *reinterpret_cast<f32x4*>(rec + 8 * c + 4) = f32x4{acc[2][0], acc[2][1], acc[3][0], acc[3][1]};
      if (c == 0) { rec[128] = m; rec[129] = l; }
    }
    if (lane == 0) ldsu[wave] = n;
    unsigned long long* dts = (unsigned long long*)(ws + (4 << 20)) + (blockIdx.x * WAVES + wave) * 4;
    { const unsigned long long dt1 = __builtin_amdgcn_s_memrealtime(); if (lane == 0) { dts[0] = dt0; dts[1] = dt1; dts[2] = dta; dts[3] = dtb; } }
    __syncthreads();
    const int t = tid & (2 * D - 1);
    const float* base = lds + (t >> 7) * (WAVES * 4 * REC);
    float M = -INFINITY, L = 0.f, o = 0.f;
    unsigned cnt = 0;
#pragma unroll
    for (int w = 0; w < WAVES * 4; ++w) M = fmaxf(M, base[w * REC + 128]);
    const float Ms = M == -INFINITY ? 0.f : M;
#pragma unroll
    for (int w = 0; w < WAVES * 4; ++w) {
      const float wt = __builtin_amdgcn_exp2f(base[w * REC + 128] - Ms);
      L = fmaf(wt, base[w * REC + 129], L);
      o = fmaf(wt, base[w * REC + (t & (D - 1))], o);
    }
#pragma unroll
    for (int w = 0; w < WAVES; ++w) cnt += ldsu[w];
    if (g != 0) {
      __builtin_amdgcn_raw_buffer_store_b128(u32x4{nosent(o), nosent(M), nosent(L), cnt}, rr, ro, g * (2 * D * 16), 16);
      return;
    }
    bool ok = true;
    if (nstat == SR) {
      u32x4 p[GW - 1];
      const unsigned long long t0 = __builtin_amdgcn_s_memrealtime();
      do {
        asm volatile("" ::: "memory");
#pragma unroll
        for (int s = 1; s < GW; ++s) p[s - 1] = __builtin_amdgcn_raw_buffer_load_b128(rr, t * 16, s * (2 * D * 16), 16);
        unsigned mxu = 0;
#pragma unroll
        for (int s = 0; s < GW - 1; ++s) mxu = max(max(max(mxu, p[s][0]), max(p[s][1], p[s][2])), p[s][3]);
        ok = __all(mxu != SENT);
      } while (!ok && __builtin_amdgcn_s_memrealtime() - t0 < KTMO);
      float Mx = M;
#pragma unroll
      for (int s = 0; s < GW - 1; ++s) { cnt += p[s][3]; Mx = fmaxf(Mx, __uint_as_float(p[s][1])); }
      ok = ok && __all(cnt == RPG * WAVES);
      const float w0 = __builtin_amdgcn_exp2f(M - Mx);
      L *= w0; o *= w0;
#pragma unroll
      for (int s = 0; s < GW - 1; ++s) {
        const float wt = __builtin_amdgcn_exp2f(__uint_as_float(p[s][1]) - Mx);
        L = fmaf(wt, __uint_as_float(p[s][2]), L);
        o = fmaf(wt, __uint_as_float(p[s][0]), o);
      }
    }
    if (ok && tid < 2 * D) out[grp * 2 * D + tid] = __float2bfloat16(o / L);
    if (lane == 0) ldsu[WAVES + wave] = ok;
    __syncthreads();
    bool all_ok = true;
#pragma unroll
    for (int w = 0; w < WAVES; ++w) all_ok = all_ok && ldsu[WAVES + w];
    if (__builtin_amdgcn_readfirstlane(all_ok)) return;
    off = 0; nstat = RPG + KLA + 2;
#pragma unroll
    for (int i = 0; i < U; ++i) Kr[i] = __builtin_amdgcn_raw_buffer_load_b128(rk, vo + i * 1024, off, 2);
#pragma unroll
    for (int i = 0; i < U; ++i) Vr[i] = __builtin_amdgcn_raw_buffer_load_b128(rv, vo + i * 1024, off, 2);
  }
}

__global__ void __launch_bounds__(64) diag(const char* __restrict__ ws, float* sink) {
  const unsigned long long* ts = (const unsigned long long*)(ws + (4 << 20));
  const int lane = threadIdx.x;
  unsigned long long mn = ~0ull, sa = 0, sb = 0;
  for (int w = lane; w < NBLK * WAVES; w += 64) { unsigned long long a = ts[4 * w]; mn = a < mn ? a : mn; sa += ts[4 * w + 2]; sb += ts[4 * w + 3]; }
  for (int o = 32; o; o >>= 1) { unsigned long long x = __shfl_xor(mn, o); mn = x < mn ? x : mn; sa += __shfl_xor(sa, o); sb += __shfl_xor(sb, o); }
  unsigned long long f = 15000;
  if (PROBE == 1) f += 10 * (sa - mn * (NBLK * WAVES)) / (NBLK * WAVES);
  if (PROBE == 2) f += 10 * (sb - mn * (NBLK * WAVES)) / (NBLK * WAVES);
  if (f > 100000) f = 100000;
  while (__builtin_amdgcn_s_memrealtime() - mn < f) __builtin_amdgcn_s_sleep(1);
  if (f == 0x7fffffffffffull) sink[lane] = 1.f;
}

extern "C" void kernel_launch(void* const* d_in, const int* in_sizes, int n_in, void* d_out, int out_size, void* d_ws,
                              size_t ws_size, hipStream_t stream) {
  attn_decode<<<NBLK, NTHR, 0, stream>>>((const char*)d_in[1], (const char*)d_in[2], (const __hip_bfloat16*)d_in[0], (char*)d_ws,
                                         (__hip_bfloat16*)d_out);
  diag<<<1, 64, 0, stream>>>((const char*)d_ws, (float*)d_ws + (3 << 20));
}
```

```cpp
#include <hip/hip_runtime.h>
#include <hip/hip_bf16.h>
#include <cstdint>

#define KU 4
#define KW 4
#define KSR 44
#define KLA 1
#define KX 24
#define KG 4
#define KTMO 50000
#define KCODE 38

constexpr int D = 128, NH = 64, SKV = 16384;
constexpr int U = KU, WAVES = KW, NTHR = WAVES * 64 + 64;
constexpr int GW = 8;
constexpr int NGRP = NH / 2, NBLK = NGRP * GW;
constexpr unsigned HB = SKV * D * 2, QB = 2 * HB;
constexpr unsigned TILE = U * 1024, RB = TILE * WAVES;
constexpr unsigned RPG = QB / RB;
constexpr unsigned SR = KSR, NPOOL = RPG - GW * SR;
constexpr unsigned POOL0 = GW * SR * RB;
constexpr unsigned SENT = 0xffffffffu;
static_assert(SR >= 2 && GW * SR <= RPG && HB % RB == 0, "");
constexpr unsigned RECB = 2 * D * 4 + 128, GRPB = 256 + GW * RECB;
constexpr int REC = 132;
constexpr int NROW = WAVES * 4;

using bf16x2 = __attribute__((ext_vector_type(2))) __bf16;
using u32x2 = __attribute__((ext_vector_type(2))) unsigned;
using u32x4 = __attribute__((ext_vector_type(4))) unsigned;
using f32x2 = __attribute__((ext_vector_type(2))) float;
using f32x4 = __attribute__((ext_vector_type(4))) float;

__device__ __forceinline__ float dot2(unsigned k, unsigned q, float acc) {
  return __builtin_amdgcn_fdot2_f32_bf16(__builtin_bit_cast(bf16x2, k), __builtin_bit_cast(bf16x2, q), acc, false);
}
template <int CTRL> __device__ __forceinline__ float dpp(float x) {
  return __builtin_bit_cast(float, __builtin_amdgcn_mov_dpp(__builtin_bit_cast(int, x), CTRL, 0xf, 0xf, true));
}
__device__ __forceinline__ unsigned nosent(float x) { const unsigned u = __float_as_uint(x); return u == SENT ? 0x7fc00000u : u; }
__device__ __forceinline__ unsigned umax4(u32x4 x) { return max(max(x[0], x[1]), max(x[2], x[3])); }
__device__ __forceinline__ unsigned pack_bf16(float lo, float hi) {
  return __builtin_bit_cast(unsigned, bf16x2{(__bf16)lo, (__bf16)hi});
}
#define SB() __builtin_amdgcn_sched_barrier(0)
#define LDS_LD(p) __hip_atomic_load((p), __ATOMIC_RELAXED, __HIP_MEMORY_SCOPE_WORKGROUP)
#define LDS_ST(p, x) __hip_atomic_store((p), (x), __ATOMIC_RELAXED, __HIP_MEMORY_SCOPE_WORKGROUP)

__global__ void __launch_bounds__(NTHR) attn_decode(const char* __restrict__ k, const char* __restrict__ v,
                                                    const __hip_bfloat16* __restrict__ q, char* __restrict__ ws,
                                                    __hip_bfloat16* __restrict__ out) {
  __shared__ __attribute__((aligned(16))) float lds[2 * NROW * REC + WAVES + 2 + NPOOL + 1];
  unsigned* const ldsu = reinterpret_cast<unsigned*>(lds + 2 * NROW * REC);
  unsigned* const verdict = ldsu + WAVES;
  unsigned* const want = verdict + 1;
  unsigned* const list = want + 1;
  const int tid = threadIdx.x, lane = tid & 63;
  unsigned pf = (unsigned)(tid - WAVES * 64) << 7;
  {
    unsigned long long pc;
    asm volatile("s_getpc_b64 %0" : "=s"(pc));
    const u32x4 rcode = {(unsigned)pc & ~255u, (unsigned)(pc >> 32) & 0xffffu, KCODE * 128u, 0x00020000u};
    asm volatile("buffer_load_dword %0, %0, %1, 0 offen" : "+v"(pf) : "s"(rcode) : "memory");
    asm volatile("" :: "s"(k), "s"(v), "s"(q), "s"(ws), "s"(out));
  }
  const int wave = __builtin_amdgcn_readfirstlane(tid >> 6);
  const int grp = blockIdx.x / GW, g = blockIdx.x % GW;
  const bool owner = g == (grp & (GW - 1));

  if (wave == WAVES) {
    for (int i = lane; i < NPOOL + 2; i += 64) want[i] = i ? SENT : 0u;
    __syncthreads();
    const auto rg = __builtin_amdgcn_make_buffer_rsrc((void*)(ws + (size_t)grp * GRPB), 0, GRPB, 0x00020000);
    const unsigned oo = 256 + lane * 16, om = 256 + 2 * D * 4 + (lane >> 5) * 16;
    const unsigned l0 = lane ? GRPB : 0;
    __hip_bfloat16* const op = out + grp * (2 * D) + lane * 4;
    if (owner) {
      __builtin_amdgcn_raw_buffer_store_b32(0u, rg, l0, 0, 16);
#pragma unroll
      for (int s = 1; s < GW; ++s) {
        const unsigned so = ((g + s) & (GW - 1)) * RECB;
        __builtin_amdgcn_raw_buffer_store_b128(u32x4{SENT, SENT, SENT, SENT}, rg, oo, so, 16);
        __builtin_amdgcn_raw_buffer_store_b128(u32x4{SENT, SENT, SENT, SENT}, rg, om, so, 16);
      }
    }
    asm volatile("s_waitcnt vmcnt(0)" : "+v"(pf) :: "memory");
    unsigned amt = KG;
    for (unsigned kk = 0;;) {
      unsigned cl = NPOOL;
      if (kk + amt <= NPOOL) {
        while (LDS_LD(want) <= kk) __builtin_amdgcn_s_sleep(1);
        cl = __builtin_amdgcn_readfirstlane(__builtin_amdgcn_raw_ptr_buffer_atomic_add_i32(amt, rg, l0, 0, 0));
        cl = min(cl, NPOOL);
      } else if (amt > 1) { amt = 1; continue; }
      if (lane < amt) LDS_ST(list + kk + lane, cl + lane < NPOOL ? POOL0 + (cl + lane) * RB : QB);
      if (cl + amt > NPOOL) break;
      kk += amt;
      amt = min(max((NPOOL - cl) / KX, 1u), (unsigned)KG);
    }
    bool solo = false;
    while (true) {
      __syncthreads();
      const float* base = lds + (lane >> 5) * (NROW * REC);
      float M = -INFINITY, L = 0.f;
      f32x4 o = {0.f, 0.f, 0.f, 0.f};
      f32x2 ml[NROW];
      f32x4 ov[NROW];
#pragma unroll
      for (int w = 0; w < NROW; ++w) ml[w] = *reinterpret_cast<const f32x2*>(base + w * REC + D);
#pragma unroll
      for (int w = 0; w < NROW; ++w) ov[w] = *reinterpret_cast<const f32x4*>(base + w * REC + (lane & 31) * 4);
      unsigned cnt = 0;
#pragma unroll
      for (int w = 0; w < WAVES; ++w) cnt += ldsu[w];
      SB();
#pragma unroll
      for (int w = 0; w < NROW; ++w) M = fmaxf(M, ml[w][0]);
      const float Ms = M == -INFINITY ? 0.f : M;
#pragma unroll
      for (int w = 0; w < NROW; ++w) {
        const float wt = __builtin_amdgcn_exp2f(ml[w][0] - Ms);
        L = fmaf(wt, ml[w][1], L);
        o += wt * ov[w];
      }
      if (!owner) {
        __builtin_amdgcn_raw_buffer_store_b128(u32x4{nosent(o[0]), nosent(o[1]), nosent(o[2]), nosent(o[3])}, rg, oo, g * RECB, 16);
        __builtin_amdgcn_raw_buffer_store_b128(u32x4{nosent(M), nosent(L), cnt, 0u}, rg, (lane & 31) ? GRPB : om, g * RECB, 16);
        return;
      }
      bool ok = true;
      if (!solo) {
        u32x4 po[GW - 1], pm[GW - 1];
        const unsigned long long t0 = __builtin_amdgcn_s_memrealtime();
        do {
          asm volatile("" ::: "memory");
          unsigned mx = 0;
#pragma unroll
          for (int s = 1; s < GW; ++s) {
            const unsigned so = ((g + s) & (GW - 1)) * RECB;
            po[s - 1] = __builtin_amdgcn_raw_buffer_load_b128(rg, oo, so, 16);
            pm[s - 1] = __builtin_amdgcn_raw_buffer_load_b128(rg, om, so, 16);
          }
#pragma unroll
          for (int s = 0; s < GW - 1; ++s) mx = max(mx, max(umax4(po[s]), umax4(pm[s])));
          ok = __all(mx != SENT);
        } while (!ok && __builtin_amdgcn_s_memrealtime() - t0 < KTMO);
        float Mx = M;
#pragma unroll
        for (int s = 0; s < GW - 1; ++s) { cnt += pm[s][2]; Mx = fmaxf(Mx, __uint_as_float(pm[s][0])); }
        ok = ok && __all(cnt == RPG * WAVES);
        const float w0 = __builtin_amdgcn_exp2f(M - Mx);
        L *= w0; o *= w0;
#pragma unroll
        for (int s = 0; s < GW - 1; ++s) {
          const float wt = __builtin_amdgcn_exp2f(__uint_as_float(pm[s][0]) - Mx);
          L = fmaf(wt, __uint_as_float(pm[s][1]), L);
          o += wt * __builtin_bit_cast(f32x4, po[s]);
        }
      }
      if (ok) *reinterpret_cast<u32x2*>(op) = u32x2{pack_bf16(o[0] / L, o[1] / L), pack_bf16(o[2] / L, o[3] / L)};
      if (lane == 0) *verdict = ok;
      __syncthreads();
      if (ok) return;
      solo = true;
    }
  }

  const auto rk = __builtin_amdgcn_make_buffer_rsrc((void*)(k + (size_t)grp * QB), 0, (int)QB, 0x00020000);
  const auto rv = __builtin_amdgcn_make_buffer_rsrc((void*)(v + (size_t)grp * QB), 0, (int)QB, 0x00020000);
  const unsigned vo = lane * 16 + wave * TILE;
  unsigned off = g * SR * RB;
  const int r = lane >> 4, c = lane & 15;
  u32x4 qa = *reinterpret_cast<const u32x4*>(q + grp * (2 * D) + c * 8);
  u32x4 qb = *reinterpret_cast<const u32x4*>(q + grp * (2 * D) + D + c * 8);
  SB();
  u32x4 Kr[U], Vr[U];
#pragma unroll
  for (int i = 0; i < U; ++i) Kr[i] = __builtin_amdgcn_raw_buffer_load_b128(rk, vo + i * 1024, off, 2);
#pragma unroll
  for (int i = 0; i < U; ++i) Vr[i] = __builtin_amdgcn_raw_buffer_load_b128(rv, vo + i * 1024, off, 2);
  SB();
  constexpr float C2 = 0.088388347648318440f * 1.4426950408889634f;
  asm volatile("" : "+v"(qa), "+v"(qb), "+v"(pf));
  unsigned nstat = SR;
  __builtin_amdgcn_s_barrier();

  while (true) {
    unsigned n = 0;
#pragma unroll 1
    for (int ph = 0; ph < 2; ++ph) {
      const u32x4 qv = ph ? qb : qa;
      const unsigned lim = ph ? QB : HB;
      float m = -INFINITY, l = 0.f;
      f32x2 acc[4];
#pragma unroll
      for (int j = 0; j < 4; ++j) acc[j] = f32x2{0.f, 0.f};
#pragma unroll 1
      while (off < lim) {
        ++n;
        if (n + KLA >= nstat && lane == 0) __hip_atomic_fetch_max(want, n + KLA + 1 - nstat, __ATOMIC_RELAXED, __HIP_MEMORY_SCOPE_WORKGROUP);
        if (n < nstat) off += RB;
        else do off = __builtin_amdgcn_readfirstlane(LDS_LD(list + (n - nstat))); while (off == SENT);
        SB();
        float s[U];
#pragma unroll
        for (int i = 0; i < U; ++i) {
          float d = dot2(Kr[i][0], qv[0], 0.f);
          d = dot2(Kr[i][1], qv[1], d); d = dot2(Kr[i][2], qv[2], d); d = dot2(Kr[i][3], qv[3], d);
          Kr[i] = __builtin_amdgcn_raw_buffer_load_b128(rk, vo + i * 1024, off, 2);
          s[i] = d;
          SB();
        }
#pragma unroll
        for (int i = 0; i < U; ++i) s[i] += dpp<0x128>(s[i]);
        SB();
#pragma unroll
        for (int i = 0; i < U; ++i) s[i] += dpp<0x124>(s[i]);
        SB();
#pragma unroll
        for (int i = 0; i < U; ++i) s[i] += dpp<0x122>(s[i]);
        SB();
#pragma unroll
        for (int i = 0; i < U; ++i) s[i] += dpp<0x121>(s[i]);
        float mx = s[0];
#pragma unroll
        for (int i = 1; i < U; ++i) mx = fmaxf(mx, s[i]);
        mx = fmaxf(m, mx * C2);
        const float alpha = __builtin_amdgcn_exp2f(m - mx);
        m = mx;
        l *= alpha;
#pragma unroll
        for (int j = 0; j < 4; ++j) acc[j] *= f32x2{alpha, alpha};
#pragma unroll
        for (int i = 0; i < U; ++i) { s[i] = __builtin_amdgcn_exp2f(fmaf(s[i], C2, -m)); l += s[i]; }
        asm volatile("" : "+v"(acc[0]), "+v"(acc[1]), "+v"(acc[2]), "+v"(acc[3]));
        SB();
#pragma unroll
        for (int i = 0; i < U; ++i) {
#pragma unroll
          for (int j = 0; j < 4; ++j)
            acc[j] = __builtin_elementwise_fma(f32x2{s[i], s[i]},
                                               f32x2{__uint_as_float(Vr[i][j] << 16), __uint_as_float(Vr[i][j] & 0xffff0000u)}, acc[j]);
          asm volatile("" : "+v"(acc[0]), "+v"(acc[1]), "+v"(acc[2]), "+v"(acc[3]) :: "memory");
          Vr[i] = __builtin_amdgcn_raw_buffer_load_b128(rv, vo + i * 1024, off, 2);
          SB();
        }
      }
      float* rec = lds + ((ph * WAVES + wave) * 4 + r) * REC;
      *reinterpret_cast<f32x4*>(rec + 8 * c) = f32x4{acc[0][0], acc[0][1], acc[1][0], acc[1][1]};
      *reinterpret_cast<f32x4*>(rec + 8 * c + 4) = f32x4{acc[2][0], acc[2][1], acc[3][0], acc[3][1]};
      if (c == 0) *reinterpret_cast<f32x2*>(rec + D) = f32x2{m, l};
    }
    if (lane == 0) ldsu[wave] = n;
    __syncthreads();
    if (!owner) return;
    __syncthreads();
    if (__builtin_amdgcn_readfirstlane(*verdict)) return;
    off = 0; nstat = RPG + KLA + 2;
#pragma unroll
    for (int i = 0; i < U; ++i) Kr[i] = __builtin_amdgcn_raw_buffer_load_b128(rk, vo + i * 1024, off, 2);
#pragma unroll
    for (int i = 0; i < U; ++i) Vr[i] = __builtin_amdgcn_raw_buffer_load_b128(rv, vo + i * 1024, off, 2);
  }
}

extern "C" void kernel_launch(void* const* d_in, const int* in_sizes, int n_in, void* d_out, int out_size, void* d_ws,
                              size_t ws_size, hipStream_t stream) {
  attn_decode<<<NBLK, NTHR, 0, stream>>>((const char*)d_in[1], (const char*)d_in[2], (const __hip_bfloat16*)d_in[0], (char*)d_ws,
                                         (__hip_bfloat16*)d_out);
}
```

```cpp
#include <hip/hip_runtime.h>
#include <hip/hip_bf16.h>
#include <cstdint>

#define KSV 28
#define KLA 1
#define KLB 1
#define KE 20
#define KX 8
#define KG 4
#define KMAP 4
#define KPACE 105
#define KP0 100
#define KTMO 50000
#define KCODE 45

constexpr int D = 128, NH = 64, SKV = 16384;
constexpr int U = 8, WAVES = 4, NTHR = WAVES * 64 + 64;
constexpr int GW = 4, NBLK = NH * GW;
constexpr unsigned HB = SKV * D * 2;
constexpr unsigned RB = U * 1024 * WAVES;
constexpr unsigned RR = HB / GW / RB;
constexpr unsigned IB = RB / 2;
constexpr unsigned SV = KSV, PR = (RR - SV) * 2, NPOOL = PR * GW;
constexpr unsigned SENT = 0xffffffffu;
static_assert(SV >= 4 && SV < RR && PR % KMAP == 0, "");
constexpr unsigned RECB = D * 4 + 128, REC0 = 128, GRPB = REC0 + GW * RECB;
constexpr int REC = 132;
constexpr int NROW = WAVES * 4;

using bf16x2 = __attribute__((ext_vector_type(2))) __bf16;
using u32x2 = __attribute__((ext_vector_type(2))) unsigned;
using u32x4 = __attribute__((ext_vector_type(4))) unsigned;
using f32x2 = __attribute__((ext_vector_type(2))) float;
using f32x4 = __attribute__((ext_vector_type(4))) float;

__device__ __forceinline__ float dot2(unsigned k, unsigned q, float acc) {
  return __builtin_amdgcn_fdot2_f32_bf16(__builtin_bit_cast(bf16x2, k), __builtin_bit_cast(bf16x2, q), acc, false);
}
template <int CTRL> __device__ __forceinline__ float dpp(float x) {
  return __builtin_bit_cast(float, __builtin_amdgcn_mov_dpp(__builtin_bit_cast(int, x), CTRL, 0xf, 0xf, true));
}
__device__ __forceinline__ unsigned nosent(float x) { const unsigned u = __float_as_uint(x); return u == SENT ? 0x7fc00000u : u; }
__device__ __forceinline__ unsigned pack_bf16(float lo, float hi) {
  return __builtin_bit_cast(unsigned, bf16x2{(__bf16)lo, (__bf16)hi});
}
#define SB() __builtin_amdgcn_sched_barrier(0)
#define LDS_LD(p) __hip_atomic_load((p), __ATOMIC_RELAXED, __HIP_MEMORY_SCOPE_WORKGROUP)
#define LDS_ST(p, x) __hip_atomic_store((p), (x), __ATOMIC_RELAXED, __HIP_MEMORY_SCOPE_WORKGROUP)
#define RSRC(p, n) __builtin_amdgcn_make_buffer_rsrc((void*)(p), 0, (int)(n), 0x00020000)

__global__ void __launch_bounds__(NTHR) attn_decode(const char* __restrict__ k, const char* __restrict__ v,
                                                    const __hip_bfloat16* __restrict__ q, char* __restrict__ ws,
                                                    __hip_bfloat16* __restrict__ out) {
  __shared__ __attribute__((aligned(16))) float lds[RR * 128 + NROW * REC + WAVES + 2 + NPOOL + 1];
  float* const sc = lds;
  float* const recs = lds + RR * 128;
  unsigned* const ldsu = reinterpret_cast<unsigned*>(recs + NROW * REC);
  unsigned* const verdict = ldsu + WAVES;
  unsigned* const want = verdict + 1;
  unsigned* const list = want + 1;
  const int tid = threadIdx.x, lane = tid & 63;
  unsigned pf = (unsigned)(tid - WAVES * 64) << 7;
  {
    unsigned long long pc;
    asm volatile("s_getpc_b64 %0" : "=s"(pc));
    const u32x4 rcode = {(unsigned)pc & ~255u, (unsigned)(pc >> 32) & 0xffffu, KCODE * 128u, 0x00020000u};
    asm volatile("buffer_load_dword %0, %0, %1, 0 offen" : "+v"(pf) : "s"(rcode) : "memory");
    asm volatile("" :: "s"(k), "s"(v), "s"(q), "s"(ws), "s"(out));
  }
  const int wave = __builtin_amdgcn_readfirstlane(tid >> 6);
  const unsigned bx = blockIdx.x, head = (bx >> 3) * 2 + ((bx >> 1) & 1), g = (bx & 1) | ((bx >> 1) & 2);
  const bool owner = g == ((bx >> 3) & (GW - 1));

  if (wave == WAVES) {
    for (int i = lane; i < NPOOL + 2; i += 64) want[i] = i ? SENT : 0u;
    __syncthreads();
    const auto rg = RSRC(ws + (size_t)head * GRPB, GRPB);
    const unsigned oo = REC0 + lane * 8, om = REC0 + D * 4;
    const unsigned l0 = lane ? GRPB : 0;
    if (owner) {
      __builtin_amdgcn_raw_buffer_store_b32(0u, rg, l0, 0, 16);
#pragma unroll
      for (int s = 1; s < GW; ++s)
        __builtin_amdgcn_raw_buffer_store_b128(u32x4{SENT, SENT, SENT, SENT}, rg, lane <= 32 ? REC0 + lane * 16 : GRPB, ((g + s) & (GW - 1)) * RECB, 16);
    }
    asm volatile("s_waitcnt vmcnt(0)" : "+v"(pf) :: "memory");
    unsigned amt = 1, hold = 0;
    for (unsigned kk = 0;;) {
      unsigned cl = NPOOL;
      if (kk + amt <= NPOOL) {
        while (LDS_LD(want) <= kk + hold) __builtin_amdgcn_s_sleep(1);
        cl = __builtin_amdgcn_readfirstlane(__builtin_amdgcn_raw_ptr_buffer_atomic_add_i32(amt, rg, l0, 0, 0));
        cl = min(cl, NPOOL);
      } else if (amt > 1) { amt = 1; continue; }
      if (lane < amt) {
        const unsigned e = cl + lane, a = e / KMAP;
        LDS_ST(list + kk + lane, e < NPOOL ? ((a % GW) * (2 * RR) + 2 * SV + (a / GW) * KMAP + e % KMAP) * IB : HB);
      }
      if (cl + amt > NPOOL) break;
      kk += amt;
      hold = cl + GW * amt + KE < NPOOL ? 0 : KLB;
      amt = min(max((NPOOL - min(cl + GW * amt, NPOOL)) / KX, 1u), (unsigned)KG);
    }
    bool solo = false;
    while (true) {
      __syncthreads();
      f32x2 ml[NROW], ov[NROW];
#pragma unroll
      for (int w = 0; w < NROW; ++w) ml[w] = *reinterpret_cast<const f32x2*>(recs + w * REC + D);
#pragma unroll
      for (int w = 0; w < NROW; ++w) ov[w] = *reinterpret_cast<const f32x2*>(recs + w * REC + lane * 2);
      unsigned cnt = 0;
#pragma unroll
      for (int w = 0; w < WAVES; ++w) cnt += ldsu[w];
      SB();
      float M = ml[0][0], L = 0.f;
      f32x2 o = {0.f, 0.f};
#pragma unroll
      for (int w = 1; w < NROW; ++w) M = fmaxf(M, ml[w][0]);
#pragma unroll
      for (int w = 0; w < NROW; ++w) {
        const float wt = __builtin_amdgcn_exp2f(ml[w][0] - M);
        L = fmaf(wt, ml[w][1], L);
        o += wt * ov[w];
      }
      if (!owner) {
        __builtin_amdgcn_raw_buffer_store_b64(u32x2{nosent(o[0]), nosent(o[1])}, rg, oo, g * RECB, 16);
        __builtin_amdgcn_raw_buffer_store_b128(u32x4{nosent(M), nosent(L), cnt, 0u}, rg, lane ? GRPB : om, g * RECB, 16);
        return;
      }
      bool ok = true;
      if (!solo) {
        u32x2 po[GW - 1];
        u32x4 pm[GW - 1];
        const unsigned long long t0 = __builtin_amdgcn_s_memrealtime();
        do {
          asm volatile("" ::: "memory");
          unsigned mx = 0;
#pragma unroll
          for (int s = 1; s < GW; ++s) {
            const unsigned so = ((g + s) & (GW - 1)) * RECB;
            po[s - 1] = __builtin_amdgcn_raw_buffer_load_b64(rg, oo, so, 16);
            pm[s - 1] = __builtin_amdgcn_raw_buffer_load_b128(rg, om, so, 16);
          }
#pragma unroll
          for (int s = 0; s < GW - 1; ++s) mx = max(max(max(mx, po[s][0]), max(po[s][1], pm[s][0])), max(pm[s][1], pm[s][2]));
          ok = __all(mx != SENT);
        } while (!ok && __builtin_amdgcn_s_memrealtime() - t0 < KTMO);
        float Mx = M;
#pragma unroll
        for (int s = 0; s < GW - 1; ++s) { cnt += pm[s][2]; Mx = fmaxf(Mx, __uint_as_float(pm[s][0])); }
        ok = ok && __all(cnt == NPOOL * WAVES);
        const float w0 = __builtin_amdgcn_exp2f(M - Mx);
        L *= w0; o *= w0;
#pragma unroll
        for (int s = 0; s < GW - 1; ++s) {
          const float wt = __builtin_amdgcn_exp2f(__uint_as_float(pm[s][0]) - Mx);
          L = fmaf(wt, __uint_as_float(pm[s][1]), L);
          o += wt * __builtin_bit_cast(f32x2, po[s]);
        }
      }
      if (ok) *reinterpret_cast<unsigned*>(out + head * D + lane * 2) = pack_bf16(o[0] / L, o[1] / L);
      if (lane == 0) *verdict = ok;
      __syncthreads();
      if (ok) return;
      solo = true;
    }
  }

  const char* const kb = k + (size_t)head * HB;
  const char* const vb = v + (size_t)head * HB;
  const unsigned vo = lane * 16 + wave * 1024;
  constexpr unsigned LS = WAVES * 1024;
  const int r = lane >> 4, c = lane & 15;
  constexpr float C2 = 0.088388347648318440f * 1.4426950408889634f;
  u32x4 R[U];
  u32x4 qv = *reinterpret_cast<const u32x4*>(q + head * D + c * 8);
#define LOADS(i_, ra_, oa_, rb_, ob_) R[i_] = (i_) < U / 2 ? __builtin_amdgcn_raw_buffer_load_b128(ra_, vo + (i_) % (U / 2) * LS, oa_, 2) \
                                                          : __builtin_amdgcn_raw_buffer_load_b128(rb_, vo + (i_) % (U / 2) * LS, ob_, 2)
#define START(reg_) do { \
    SB(); \
    const auto rk_ = RSRC(kb, HB); \
    _Pragma("unroll") for (int i = 0; i < U; ++i) { LOADS(i, rk_, (reg_) * (RR * RB), rk_, (reg_) * (RR * RB) + IB); SB(); } \
  } while (0)
#define PV(i_, p_) do { \
    _Pragma("unroll") for (int x = 0; x < 4; ++x) \
      acc[x] = __builtin_elementwise_fma(f32x2{p_, p_}, f32x2{__uint_as_float(R[i_][x] << 16), __uint_as_float(R[i_][x] & 0xffff0000u)}, acc[x]); \
    asm volatile("" : "+v"(acc[0]), "+v"(acc[1]), "+v"(acc[2]), "+v"(acc[3]) :: "memory"); \
  } while (0)
#define QK(i_) dot2(R[i_][3], qv[3], dot2(R[i_][2], qv[2], dot2(R[i_][1], qv[1], dot2(R[i_][0], qv[0], 0.f))))
#define PACE() do { if (KPACE) { due += KPACE; while ((int)((unsigned)__builtin_amdgcn_s_memrealtime() - due) < 0) __builtin_amdgcn_s_sleep(1); } } while (0)
  START(g);
  asm volatile("" : "+v"(qv), "+v"(pf));
  __builtin_amdgcn_s_barrier();
  unsigned due = (unsigned)__builtin_amdgcn_s_memrealtime() - KP0;
  unsigned reg = g, nreg = 1, nstat = SV;
  const auto rk = RSRC(kb, HB);
  const auto rv = RSRC(vb, HB);

  while (true) {
    float m = -3.0e38f, l = 0.f;
    f32x2 acc[4];
#pragma unroll
    for (int j = 0; j < 4; ++j) acc[j] = f32x2{0.f, 0.f};
    unsigned n = 0, cur = HB;
#pragma unroll 1
    while (true) {
      const unsigned rb = reg * (RR * RB);
#pragma unroll 1
      for (unsigned j = 0; j < nstat; ++j) {
        const bool last = j + 1 == nstat;
        PACE();
        const auto rn = RSRC(last ? vb : kb, HB);
        const unsigned off = rb + (last ? 0 : j + 1) * RB;
        SB();
        float s[U];
#pragma unroll
        for (int i = 0; i < U; ++i) {
          s[i] = QK(i);
          LOADS(i, rn, off, rn, off + IB);
          SB();
        }
#pragma unroll
        for (int i = 0; i < 4; ++i) { const float a = s[i] + dpp<0x128>(s[i]), b = s[i + 4] + dpp<0x128>(s[i + 4]); s[i] = (c & 8) ? b : a; }
#pragma unroll
        for (int i = 0; i < 2; ++i) { const float a = s[i] + dpp<0x141>(s[i]), b = s[i + 2] + dpp<0x141>(s[i + 2]); s[i] = (c & 4) ? b : a; }
        { const float a = s[0] + dpp<0x4e>(s[0]), b = s[1] + dpp<0x4e>(s[1]); s[0] = (c & 2) ? b : a; }
        s[0] += dpp<0xb1>(s[0]);
        sc[j * 128 + wave * 32 + (lane >> 1)] = s[0] * C2;
      }
#pragma unroll 1
      for (unsigned j = 0; j < nstat; ++j) {
        const bool last = j + 1 == nstat;
        PACE();
        unsigned oa = rb + (j + 1) * RB, ob = oa + IB;
        if (nstat == SV) {
          if (j + 1 + KLA + KLB >= SV && lane == 0) __hip_atomic_fetch_max(want, j + 2 + KLA + KLB - SV, __ATOMIC_RELAXED, __HIP_MEMORY_SCOPE_WORKGROUP);
          if (last) do cur = __builtin_amdgcn_readfirstlane(LDS_LD(list)); while (cur == SENT);
        }
        if (last) oa = ob = cur;
        const auto rn = RSRC(last ? kb : vb, HB);
        const f32x4 sa = *reinterpret_cast<const f32x4*>(sc + j * 128 + wave * 32 + r * 8);
        const f32x4 sb = *reinterpret_cast<const f32x4*>(sc + j * 128 + wave * 32 + r * 8 + 4);
        float s[U] = {sa[0], sa[1], sa[2], sa[3], sb[0], sb[1], sb[2], sb[3]};
        float mx = m;
#pragma unroll
        for (int i = 0; i < U; ++i) mx = fmaxf(mx, s[i]);
        const float alpha = __builtin_amdgcn_exp2f(m - mx);
        m = mx;
        l *= alpha;
#pragma unroll
        for (int i = 0; i < 4; ++i) acc[i] *= f32x2{alpha, alpha};
#pragma unroll
        for (int i = 0; i < U; ++i) { s[i] = __builtin_amdgcn_exp2f(s[i] - m); l += s[i]; }
        asm volatile("" : "+v"(acc[0]), "+v"(acc[1]), "+v"(acc[2]), "+v"(acc[3]), "+v"(l) :: "memory");
        SB();
#pragma unroll
        for (int i = 0; i < U; ++i) {
          PV(i, s[i]);
          LOADS(i, rn, oa, rv, ob);
          SB();
        }
      }
      if (--nreg == 0) break;
      ++reg;
      START(reg);
    }
#pragma unroll 1
    while (cur < HB) {
      ++n;
      if (lane == 0) __hip_atomic_fetch_max(want, n + 1 + KLA + KLB, __ATOMIC_RELAXED, __HIP_MEMORY_SCOPE_WORKGROUP);
      do cur = __builtin_amdgcn_readfirstlane(LDS_LD(list + n)); while (cur == SENT);
      SB();
      float s[U / 2];
#pragma unroll
      for (int i = 0; i < U / 2; ++i) {
        s[i] = QK(i);
        LOADS(i, rk, cur, rv, cur);
        SB();
      }
#pragma unroll
      for (int i = 0; i < U / 2; ++i) s[i] += dpp<0x128>(s[i]);
      SB();
#pragma unroll
      for (int i = 0; i < U / 2; ++i) s[i] += dpp<0x124>(s[i]);
      SB();
#pragma unroll
      for (int i = 0; i < U / 2; ++i) s[i] += dpp<0x122>(s[i]);
      SB();
#pragma unroll
      for (int i = 0; i < U / 2; ++i) s[i] += dpp<0x121>(s[i]);
      float mx = s[0];
#pragma unroll
      for (int i = 1; i < U / 2; ++i) mx = fmaxf(mx, s[i]);
      mx = fmaxf(m, mx * C2);
      const float alpha = __builtin_amdgcn_exp2f(m - mx);
      m = mx;
      l *= alpha;
#pragma unroll
      for (int i = 0; i < 4; ++i) acc[i] *= f32x2{alpha, alpha};
#pragma unroll
      for (int i = 0; i < U / 2; ++i) { s[i] = __builtin_amdgcn_exp2f(fmaf(s[i], C2, -m)); l += s[i]; }
      asm volatile("" : "+v"(acc[0]), "+v"(acc[1]), "+v"(acc[2]), "+v"(acc[3]), "+v"(l) :: "memory");
      SB();
#pragma unroll
      for (int i = U / 2; i < U; ++i) {
        PV(i, s[i - U / 2]);
        LOADS(i, rk, cur, rv, cur);
        SB();
      }
    }
    float* rec = recs + (wave * 4 + r) * REC;
    *reinterpret_cast<f32x4*>(rec + 8 * c) = f32x4{acc[0][0], acc[0][1], acc[1][0], acc[1][1]};
    *reinterpret_cast<f32x4*>(rec + 8 * c + 4) = f32x4{acc[2][0], acc[2][1], acc[3][0], acc[3][1]};
    if (c == 0) *reinterpret_cast<f32x2*>(rec + D) = f32x2{m, l};
    if (lane == 0) ldsu[wave] = n;
    __syncthreads();
    if (!owner) return;
    __syncthreads();
    if (__builtin_amdgcn_readfirstlane(*verdict)) return;
    reg = 0; nreg = GW; nstat = RR;
    START(0u);
  }
}

extern "C" void kernel_launch(void* const* d_in, const int* in_sizes, int n_in, void* d_out, int out_size, void* d_ws,
                              size_t ws_size, hipStream_t stream) {
  attn_decode<<<NBLK, NTHR, 0, stream>>>((const char*)d_in[1], (const char*)d_in[2], (const __hip_bfloat16*)d_in[0], (char*)d_ws,
                                         (__hip_bfloat16*)d_out);
}
```

```cpp
#include <hip/hip_runtime.h>
#include <hip/hip_bf16.h>
#include <cstdint>

#define KPQ 216
#define KBANK 40
#define KFREE 1000
#define KTMO 50000

constexpr int D = 128, NH = 64, SKV = 16384;
constexpr int U = 8, WAVES = 4, NTHR = WAVES * 64 + 64;
constexpr int GW = 4, NBLK = NH * GW;
constexpr unsigned HB = SKV * D * 2;
constexpr unsigned RB = U * 1024 * WAVES;
constexpr unsigned RR = HB / GW / RB;
constexpr unsigned SENT = 0xffffffffu;
static_assert(RR == 32, "");
constexpr unsigned RECB = D * 4 + 128, GRPB = GW * RECB;
constexpr int REC = 132;
constexpr int NROW = WAVES * 4;

using bf16x2 = __attribute__((ext_vector_type(2))) __bf16;
using u32x2 = __attribute__((ext_vector_type(2))) unsigned;
using u32x4 = __attribute__((ext_vector_type(4))) unsigned;
using f32x2 = __attribute__((ext_vector_type(2))) float;
using f32x4 = __attribute__((ext_vector_type(4))) float;

__device__ __forceinline__ float dot2(unsigned k, unsigned q, float acc) {
  return __builtin_amdgcn_fdot2_f32_bf16(__builtin_bit_cast(bf16x2, k), __builtin_bit_cast(bf16x2, q), acc, false);
}
template <int CTRL> __device__ __forceinline__ float dpp(float x) {
  return __builtin_bit_cast(float, __builtin_amdgcn_mov_dpp(__builtin_bit_cast(int, x), CTRL, 0xf, 0xf, true));
}
__device__ __forceinline__ unsigned nosent(float x) { const unsigned u = __float_as_uint(x); return u == SENT ? 0x7fc00000u : u; }
__device__ __forceinline__ unsigned pack_bf16(float lo, float hi) {
  return __builtin_bit_cast(unsigned, bf16x2{(__bf16)lo, (__bf16)hi});
}
__device__ __forceinline__ unsigned roff(unsigned g, unsigned j) {
  return (((g >> 1) << 6) | ((j >> 1) << 2) | ((g & 1) << 1) | (j & 1)) * RB;
}
__device__ __forceinline__ unsigned clk4() { return (unsigned)__builtin_amdgcn_s_memrealtime() << 2; }
#define SB() __builtin_amdgcn_sched_barrier(0)
#define RSRC(p, n) __builtin_amdgcn_make_buffer_rsrc((void*)(p), 0, (int)(n), 0x00020000)
constexpr int NT = 2, SC1 = 16;

__global__ void __launch_bounds__(NTHR) attn_decode(const char* __restrict__ k, const char* __restrict__ v,
                                                    const __hip_bfloat16* __restrict__ q, char* __restrict__ ws,
                                                    __hip_bfloat16* __restrict__ out) {
  __shared__ __attribute__((aligned(16))) float lds[RR * 128 + NROW * REC + 4];
  float* const sc = lds;
  float* const recs = lds + RR * 128;
  unsigned* const verdict = reinterpret_cast<unsigned*>(recs + NROW * REC);
  const int tid = threadIdx.x, lane = tid & 63;
  asm volatile("" :: "s"(k), "s"(v), "s"(q), "s"(ws), "s"(out));
  const int wave = __builtin_amdgcn_readfirstlane(tid >> 6);
  const unsigned bx = blockIdx.x, g = ((bx >> 3) & 1) | ((bx >> 1) & 2), head = ((bx >> 4) << 2) | (bx & 3);
  const bool owner = g == (bx >> 6);

  if (wave == WAVES) {
    const auto rg = RSRC(ws + (size_t)head * GRPB, GRPB);
    if (owner) {
#pragma unroll
      for (int s = 1; s < GW; ++s)
        __builtin_amdgcn_raw_buffer_store_b128(u32x4{SENT, SENT, SENT, SENT}, rg, lane <= 32 ? lane * 16 : GRPB, ((g + s) & (GW - 1)) * RECB, SC1);
      asm volatile("s_waitcnt vmcnt(0)" ::: "memory");
    }
    bool solo = false;
    while (true) {
      __syncthreads();
      f32x2 ml[NROW], ov[NROW];
#pragma unroll
      for (int w = 0; w < NROW; ++w) ml[w] = *reinterpret_cast<const f32x2*>(recs + w * REC + D);
#pragma unroll
      for (int w = 0; w < NROW; ++w) ov[w] = *reinterpret_cast<const f32x2*>(recs + w * REC + lane * 2);
      SB();
      float M = ml[0][0], L = 0.f;
      f32x2 o = {0.f, 0.f};
#pragma unroll
      for (int w = 1; w < NROW; ++w) M = fmaxf(M, ml[w][0]);
#pragma unroll
      for (int w = 0; w < NROW; ++w) {
        const float wt = __builtin_amdgcn_exp2f(ml[w][0] - M);
        L = fmaf(wt, ml[w][1], L);
        o += wt * ov[w];
      }
      if (!owner) {
        __builtin_amdgcn_raw_buffer_store_b64(u32x2{nosent(o[0]), nosent(o[1])}, rg, lane * 8, g * RECB, SC1);
        __builtin_amdgcn_raw_buffer_store_b64(u32x2{nosent(M), nosent(L)}, rg, lane ? GRPB : D * 4, g * RECB, SC1);
        return;
      }
      bool ok = true;
      if (!solo) {
        u32x2 po[GW - 1], pm[GW - 1];
        const unsigned long long t0 = __builtin_amdgcn_s_memrealtime();
        do {
          asm volatile("" ::: "memory");
          unsigned mx = 0;
#pragma unroll
          for (int s = 1; s < GW; ++s) {
            const unsigned so = ((g + s) & (GW - 1)) * RECB;
            po[s - 1] = __builtin_amdgcn_raw_buffer_load_b64(rg, lane * 8, so, SC1);
            pm[s - 1] = __builtin_amdgcn_raw_buffer_load_b64(rg, D * 4, so, SC1);
          }
#pragma unroll
          for (int s = 0; s < GW - 1; ++s) mx = max(max(mx, max(po[s][0], po[s][1])), max(pm[s][0], pm[s][1]));
          ok = __all(mx != SENT);
        } while (!ok && __builtin_amdgcn_s_memrealtime() - t0 < KTMO);
        float Mx = M;
#pragma unroll
        for (int s = 0; s < GW - 1; ++s) Mx = fmaxf(Mx, __uint_as_float(pm[s][0]));
        const float w0 = __builtin_amdgcn_exp2f(M - Mx);
        L *= w0; o *= w0;
#pragma unroll
        for (int s = 0; s < GW - 1; ++s) {
          const float wt = __builtin_amdgcn_exp2f(__uint_as_float(pm[s][0]) - Mx);
          L = fmaf(wt, __uint_as_float(pm[s][1]), L);
          o += wt * __builtin_bit_cast(f32x2, po[s]);
        }
      }
      if (ok) *reinterpret_cast<unsigned*>(out + head * D + lane * 2) = pack_bf16(o[0] / L, o[1] / L);
      if (lane == 0) *verdict = ok;
      __syncthreads();
      if (ok) return;
      solo = true;
    }
  }

  const char* const kb = k + (size_t)head * HB;
  const char* const vb = v + (size_t)head * HB;
  const unsigned vo = lane * 16 + wave * 1024, xr = ((bx >> 4) & 1) << 13;
  const int r = lane >> 4, c = lane & 15;
  constexpr float C2 = 0.088388347648318440f * 1.4426950408889634f;
  u32x4 R[U];
  u32x4 qv = *reinterpret_cast<const u32x4*>(q + head * D + c * 8);
#define LOADS(i_, r_, o_) do { \
    R[i_] = __builtin_amdgcn_raw_buffer_load_b128(r_, (vo + (i_) % 4 * (WAVES * 1024)) ^ xr, (o_) + (i_) / 4 * (RB / 2), NT); \
    SB(); \
  } while (0)
#define START(g_) do { \
    SB(); \
    const auto rk_ = RSRC(kb, HB); \
    _Pragma("unroll") for (int i = 0; i < U; ++i) LOADS(i, rk_, roff(g_, 0)); \
  } while (0)
#define PACE() do { \
    due += KPQ; \
    if ((int)(due - tfree) > 0) due = tfree; \
    int lag_; \
    while ((lag_ = (int)(clk4() - due)) < 0) __builtin_amdgcn_s_sleep(1); \
    due += max(lag_ - KBANK, 0); \
  } while (0)
  START(g);
  asm volatile("" : "+v"(qv));
  unsigned due = clk4() - 200 + wave * (KPQ / 4);
  const unsigned tfree = due + 4 * RR * KPQ - KFREE;
  unsigned reg = g, next = GW;

  while (true) {
    float m = -3.0e38f, l = 0.f;
    f32x2 acc[4];
#pragma unroll
    for (int j = 0; j < 4; ++j) acc[j] = f32x2{0.f, 0.f};
#pragma unroll 1
    while (true) {
#pragma unroll 1
      for (unsigned j = 0; j < RR; ++j) {
        const bool last = j + 1 == RR;
        const auto rn = RSRC(last ? vb : kb, HB);
        const unsigned off = roff(reg, last ? 0 : j + 1);
        float s[U];
#pragma unroll
        for (int i = 0; i < U; ++i) {
          if (i % 4 == 0) PACE();
          s[i] = dot2(R[i][3], qv[3], dot2(R[i][2], qv[2], dot2(R[i][1], qv[1], dot2(R[i][0], qv[0], 0.f))));
          asm volatile("" : "+v"(s[i]) :: "memory");
          LOADS(i, rn, off);
        }
#pragma unroll
        for (int i = 0; i < 4; ++i) { const float a = s[i] + dpp<0x128>(s[i]), b = s[i + 4] + dpp<0x128>(s[i + 4]); s[i] = (c & 8) ? b : a; }
#pragma unroll
        for (int i = 0; i < 2; ++i) { const float a = s[i] + dpp<0x141>(s[i]), b = s[i + 2] + dpp<0x141>(s[i + 2]); s[i] = (c & 4) ? b : a; }
        { const float a = s[0] + dpp<0x4e>(s[0]), b = s[1] + dpp<0x4e>(s[1]); s[0] = (c & 2) ? b : a; }
        s[0] += dpp<0xb1>(s[0]);
        sc[j * 128 + wave * 32 + (lane >> 1)] = s[0] * C2;
      }
#pragma unroll 1
      for (unsigned j = 0; j < RR; ++j) {
        const bool last = j + 1 == RR;
        const auto rn = RSRC(last ? kb : vb, HB);
        const unsigned off = last ? roff(next, 0) : roff(reg, j + 1);
        const f32x4 sa = *reinterpret_cast<const f32x4*>(sc + j * 128 + wave * 32 + r * 8);
        const f32x4 sb = *reinterpret_cast<const f32x4*>(sc + j * 128 + wave * 32 + r * 8 + 4);
        float s[U] = {sa[0], sa[1], sa[2], sa[3], sb[0], sb[1], sb[2], sb[3]};
        float mx = m;
#pragma unroll
        for (int i = 0; i < U; ++i) mx = fmaxf(mx, s[i]);
        const float alpha = __builtin_amdgcn_exp2f(m - mx);
        m = mx;
        l *= alpha;
#pragma unroll
        for (int i = 0; i < 4; ++i) acc[i] *= f32x2{alpha, alpha};
#pragma unroll
        for (int i = 0; i < U; ++i) { s[i] = __builtin_amdgcn_exp2f(s[i] - m); l += s[i]; }
        asm volatile("" : "+v"(acc[0]), "+v"(acc[1]), "+v"(acc[2]), "+v"(acc[3]), "+v"(l) :: "memory");
        SB();
#pragma unroll
        for (int i = 0; i < U; ++i) {
          if (i % 4 == 0) PACE();
#pragma unroll
          for (int x = 0; x < 4; ++x)
            acc[x] = __builtin_elementwise_fma(f32x2{s[i], s[i]},
                                               f32x2{__uint_as_float(R[i][x] << 16), __uint_as_float(R[i][x] & 0xffff0000u)}, acc[x]);
          asm volatile("" : "+v"(acc[0]), "+v"(acc[1]), "+v"(acc[2]), "+v"(acc[3]) :: "memory");
          LOADS(i, rn, off);
        }
      }
      if (next == GW) break;
      reg = next++;
    }
    float* rec = recs + (wave * 4 + r) * REC;
    *reinterpret_cast<f32x4*>(rec + 8 * c) = f32x4{acc[0][0], acc[0][1], acc[1][0], acc[1][1]};
    *reinterpret_cast<f32x4*>(rec + 8 * c + 4) = f32x4{acc[2][0], acc[2][1], acc[3][0], acc[3][1]};
    if (c == 0) *reinterpret_cast<f32x2*>(rec + D) = f32x2{m, l};
    __syncthreads();
    if (!owner) return;
    __syncthreads();
    if (__builtin_amdgcn_readfirstlane(*verdict)) return;
    reg = 0; next = 1;
    START(0u);
  }
}

extern "C" void kernel_launch(void* const* d_in, const int* in_sizes, int n_in, void* d_out, int out_size, void* d_ws,
                              size_t ws_size, hipStream_t stream) {
  attn_decode<<<NBLK, NTHR, 0, stream>>>((const char*)d_in[1], (const char*)d_in[2], (const __hip_bfloat16*)d_in[0], (char*)d_ws,
                                         (__hip_bfloat16*)d_out);
}
```

```cpp
#include <hip/hip_runtime.h>
#include <hip/hip_bf16.h>
#include <cstdint>

constexpr int KPQ = 210;
constexpr int KT = 110;
constexpr int KG = 3;
constexpr int KPN = 213;
constexpr int KBANK = 40;
constexpr int KFREE = 1000;
constexpr int KTMO = 50000;

constexpr int D = 128, NH = 64, SKV = 16384;
constexpr int U = 8, WAVES = 4, NTHR = WAVES * 64 + 64;
constexpr int GW = 4, NBLK = NH * GW;
constexpr unsigned HB = SKV * D * 2;
constexpr unsigned RB = U * 1024 * WAVES;
constexpr unsigned RR = HB / GW / RB;
constexpr unsigned SENT = 0xffffffffu;
static_assert(RR == 32, "");
constexpr unsigned RECB = D * 4 + 128, GRPB = GW * RECB;
constexpr int REC = 132;
constexpr int NROW = WAVES * 4;

using bf16x2 = __attribute__((ext_vector_type(2))) __bf16;
using u32x2 = __attribute__((ext_vector_type(2))) unsigned;
using u32x4 = __attribute__((ext_vector_type(4))) unsigned;
using f32x2 = __attribute__((ext_vector_type(2))) float;
using f32x4 = __attribute__((ext_vector_type(4))) float;

__device__ __forceinline__ float dot2(unsigned k, unsigned q, float acc) {
  return __builtin_amdgcn_fdot2_f32_bf16(__builtin_bit_cast(bf16x2, k), __builtin_bit_cast(bf16x2, q), acc, false);
}
template <int CTRL> __device__ __forceinline__ float dpp(float x) {
  return __builtin_bit_cast(float, __builtin_amdgcn_mov_dpp(__builtin_bit_cast(int, x), CTRL, 0xf, 0xf, true));
}
__device__ __forceinline__ unsigned nosent(float x) { const unsigned u = __float_as_uint(x); return u == SENT ? 0x7fc00000u : u; }
__device__ __forceinline__ unsigned pack_bf16(float lo, float hi) {
  return __builtin_bit_cast(unsigned, bf16x2{(__bf16)lo, (__bf16)hi});
}
__device__ __forceinline__ unsigned roff(unsigned g, unsigned j) {
  return ((g >> 1) << 21) | ((j >> 1) << 17) | ((g & 1) << 15) | ((j & 1) << 14);
}
__device__ __forceinline__ unsigned clk4() { return (unsigned)__builtin_amdgcn_s_memrealtime() << 2; }
#define SB() __builtin_amdgcn_sched_barrier(0)
#define RSRC(p, n) __builtin_amdgcn_make_buffer_rsrc((void*)(p), 0, (int)(n), 0x00020000)
constexpr int NT = 2, SC1 = 16;

__global__ void __launch_bounds__(NTHR) attn_decode(const char* __restrict__ k, const char* __restrict__ v,
                                                    const __hip_bfloat16* __restrict__ q, char* __restrict__ ws,
                                                    __hip_bfloat16* __restrict__ out) {
  __shared__ __attribute__((aligned(16))) float lds[RR * 128 + NROW * REC + 4];
  float* const sc = lds;
  float* const recs = lds + RR * 128;
  unsigned* const verdict = reinterpret_cast<unsigned*>(recs + NROW * REC);
  const int tid = threadIdx.x, lane = tid & 63;
  asm volatile("" :: "s"(k), "s"(v), "s"(q), "s"(ws), "s"(out));
  const int wave = __builtin_amdgcn_readfirstlane(tid >> 6);
  const unsigned bx = blockIdx.x, g = ((bx >> 3) & 1) | ((bx >> 1) & 2), head = ((bx >> 4) << 2) | (bx & 3);
  const bool owner = g == (bx >> 6);

  if (wave == WAVES) {
    const auto rg = RSRC(ws + (size_t)head * GRPB, GRPB);
    if (owner) {
#pragma unroll
      for (int s = 1; s < GW; ++s)
        __builtin_amdgcn_raw_buffer_store_b128(u32x4{SENT, SENT, SENT, SENT}, rg, lane <= 32 ? lane * 16 : GRPB, ((g + s) & (GW - 1)) * RECB, SC1);
      asm volatile("s_waitcnt vmcnt(0)" ::: "memory");
    }
    bool solo = false;
    while (true) {
      __syncthreads();
      f32x2 ml[NROW], ov[NROW];
#pragma unroll
      for (int w = 0; w < NROW; ++w) ml[w] = *reinterpret_cast<const f32x2*>(recs + w * REC + D);
#pragma unroll
      for (int w = 0; w < NROW; ++w) ov[w] = *reinterpret_cast<const f32x2*>(recs + w * REC + lane * 2);
      SB();
      float M = ml[0][0], L = 0.f;
      f32x2 o = {0.f, 0.f};
#pragma unroll
      for (int w = 1; w < NROW; ++w) M = fmaxf(M, ml[w][0]);
#pragma unroll
      for (int w = 0; w < NROW; ++w) {
        const float wt = __builtin_amdgcn_exp2f(ml[w][0] - M);
        L = fmaf(wt, ml[w][1], L);
        o += wt * ov[w];
      }
      if (!owner) {
        __builtin_amdgcn_raw_buffer_store_b64(u32x2{nosent(o[0]), nosent(o[1])}, rg, lane * 8, g * RECB, SC1);
        __builtin_amdgcn_raw_buffer_store_b64(u32x2{nosent(M), nosent(L)}, rg, lane ? GRPB : D * 4, g * RECB, SC1);
        return;
      }
      bool ok = true;
      if (!solo) {
        u32x2 po[GW - 1], pm[GW - 1];
        const unsigned long long t0 = __builtin_amdgcn_s_memrealtime();
        do {
          asm volatile("" ::: "memory");
          unsigned mx = 0;
#pragma unroll
          for (int s = 1; s < GW; ++s) {
            const unsigned so = ((g + s) & (GW - 1)) * RECB;
            po[s - 1] = __builtin_amdgcn_raw_buffer_load_b64(rg, lane * 8, so, SC1);
            pm[s - 1] = __builtin_amdgcn_raw_buffer_load_b64(rg, D * 4, so, SC1);
          }
#pragma unroll
          for (int s = 0; s < GW - 1; ++s) mx = max(max(mx, max(po[s][0], po[s][1])), max(pm[s][0], pm[s][1]));
          ok = __all(mx != SENT);
        } while (!ok && __builtin_amdgcn_s_memrealtime() - t0 < KTMO);
        float Mx = M;
#pragma unroll
        for (int s = 0; s < GW - 1; ++s) Mx = fmaxf(Mx, __uint_as_float(pm[s][0]));
        const float w0 = __builtin_amdgcn_exp2f(M - Mx);
        L *= w0; o *= w0;
#pragma unroll
        for (int s = 0; s < GW - 1; ++s) {
          const float wt = __builtin_amdgcn_exp2f(__uint_as_float(pm[s][0]) - Mx);
          L = fmaf(wt, __uint_as_float(pm[s][1]), L);
          o += wt * __builtin_bit_cast(f32x2, po[s]);
        }
      }
      if (ok) *reinterpret_cast<unsigned*>(out + head * D + lane * 2) = pack_bf16(o[0] / L, o[1] / L);
      if (lane == 0) *verdict = ok;
      __syncthreads();
      if (ok) return;
      solo = true;
    }
  }

  const char* const kb = k + (size_t)head * HB;
  const char* const vb = v + (size_t)head * HB;
  const unsigned vo = lane * 16 + wave * 1024, xr = (((bx >> 2) ^ (bx >> 4)) & 1) << 13;
  const int r = lane >> 4, c = lane & 15;
  constexpr float C2 = 0.088388347648318440f * 1.4426950408889634f;
  u32x4 R[U];
  u32x4 qv = *reinterpret_cast<const u32x4*>(q + head * D + c * 8);
#define LOADS(i_, r_, o_) do { \
    R[i_] = __builtin_amdgcn_raw_buffer_load_b128(r_, (vo + (((i_) & 1) << 13 | ((i_) & 2) << 11)) ^ xr, (o_) + ((i_) / 4 << 16), NT); \
    SB(); \
  } while (0)
#define START(g_) do { \
    SB(); \
    const auto rk_ = RSRC(kb, HB); \
    _Pragma("unroll") for (int i = 0; i < U; ++i) LOADS(i, rk_, roff(g_, 0)); \
  } while (0)
#define PACE() do {                              \
    unsigned now_ = clk4(); \
    due += KPQ; \
    due += max(KT - max((int)(due - now_), 0), 0) >> KG;     \
    if ((int)(due - tfree) > 0) due = tfree; \
    while ((int)(now_ - due) < 0) { __builtin_amdgcn_s_sleep(1); now_ = clk4(); } \
    due += max((int)(now_ - due) - KBANK, 0); \
    SB(); \
  } while (0)
  START(g);
  asm volatile("" : "+v"(qv));
  unsigned due = clk4() - 200 + wave * (KPQ / 4);
  const unsigned tfree = due + 4 * RR * KPN - KFREE;
  unsigned reg = g, next = GW;

  while (true) {
    float m = -3.0e38f, l = 0.f;
    f32x2 acc[4];
#pragma unroll
    for (int j = 0; j < 4; ++j) acc[j] = f32x2{0.f, 0.f};
#pragma unroll 1
    while (true) {
#pragma unroll 1
      for (unsigned j = 0; j < RR; ++j) {
        const bool last = j + 1 == RR;
        const auto rn = RSRC(last ? vb : kb, HB);
        const unsigned off = roff(reg, last ? 0 : j + 1);
        float s[U];
#pragma unroll
        for (int h = 0; h < U; h += 4) {
#pragma unroll
          for (int i = h; i < h + 4; ++i) {
            s[i] = dot2(R[i][3], qv[3], dot2(R[i][2], qv[2], dot2(R[i][1], qv[1], dot2(R[i][0], qv[0], 0.f))));
            asm volatile("" : "+v"(s[i]) :: "memory");
          }
          PACE();
#pragma unroll
          for (int i = h; i < h + 4; ++i) LOADS(i, rn, off);
        }
#pragma unroll
        for (int i = 0; i < 4; ++i) { const float a = s[i] + dpp<0x128>(s[i]), b = s[i + 4] + dpp<0x128>(s[i + 4]); s[i] = (c & 8) ? b : a; }
#pragma unroll
        for (int i = 0; i < 2; ++i) { const float a = s[i] + dpp<0x141>(s[i]), b = s[i + 2] + dpp<0x141>(s[i + 2]); s[i] = (c & 4) ? b : a; }
        { const float a = s[0] + dpp<0x4e>(s[0]), b = s[1] + dpp<0x4e>(s[1]); s[0] = (c & 2) ? b : a; }
        s[0] += dpp<0xb1>(s[0]);
        sc[j * 128 + wave * 32 + (lane >> 1)] = s[0] * C2;
      }
#pragma unroll 1
      for (unsigned j = 0; j < RR; ++j) {
        const bool last = j + 1 == RR;
        const auto rn = RSRC(last ? kb : vb, HB);
        const unsigned off = last ? roff(next, 0) : roff(reg, j + 1);
        const f32x4 sa = *reinterpret_cast<const f32x4*>(sc + j * 128 + wave * 32 + r * 8);
        const f32x4 sb = *reinterpret_cast<const f32x4*>(sc + j * 128 + wave * 32 + r * 8 + 4);
        float s[U] = {sa[0], sa[1], sa[2], sa[3], sb[0], sb[1], sb[2], sb[3]};
        float mx = m;
#pragma unroll
        for (int i = 0; i < U; ++i) mx = fmaxf(mx, s[i]);
        const float alpha = __builtin_amdgcn_exp2f(m - mx);
        m = mx;
        l *= alpha;
#pragma unroll
        for (int i = 0; i < 4; ++i) acc[i] *= f32x2{alpha, alpha};
#pragma unroll
        for (int i = 0; i < U; ++i) { s[i] = __builtin_amdgcn_exp2f(s[i] - m); l += s[i]; }
        asm volatile("" : "+v"(acc[0]), "+v"(acc[1]), "+v"(acc[2]), "+v"(acc[3]), "+v"(l) :: "memory");
        SB();
#pragma unroll
        for (int h = 0; h < U; h += 4) {
#pragma unroll
          for (int i = h; i < h + 4; ++i) {
#pragma unroll
            for (int x = 0; x < 4; ++x)
              acc[x] = __builtin_elementwise_fma(f32x2{s[i], s[i]},
                                                 f32x2{__uint_as_float(R[i][x] << 16), __uint_as_float(R[i][x] & 0xffff0000u)}, acc[x]);
            asm volatile("" : "+v"(acc[0]), "+v"(acc[1]), "+v"(acc[2]), "+v"(acc[3]) :: "memory");
          }
          if (!last) PACE();
#pragma unroll
          for (int i = h; i < h + 4; ++i) LOADS(i, rn, off);
        }
      }
      if (next == GW) break;
      reg = next++;
    }
    float* rec = recs + (wave * 4 + r) * REC;
    *reinterpret_cast<f32x4*>(rec + 8 * c) = f32x4{acc[0][0], acc[0][1], acc[1][0], acc[1][1]};
    *reinterpret_cast<f32x4*>(rec + 8 * c + 4) = f32x4{acc[2][0], acc[2][1], acc[3][0], acc[3][1]};
    if (c == 0) *reinterpret_cast<f32x2*>(rec + D) = f32x2{m, l};
    __syncthreads();
    if (!owner) return;
    __syncthreads();
    if (__builtin_amdgcn_readfirstlane(*verdict)) return;
    reg = 0; next = 1;
    START(0u);
  }
}

extern "C" void kernel_launch(void* const* d_in, const int* in_sizes, int n_in, void* d_out, int out_size, void* d_ws,
                              size_t ws_size, hipStream_t stream) {
  attn_decode<<<NBLK, NTHR, 0, stream>>>((const char*)d_in[1], (const char*)d_in[2], (const __hip_bfloat16*)d_in[0], (char*)d_ws,
                                         (__hip_bfloat16*)d_out);
}
```
